```python
import torch

def _softmax(x, axis=-1):
    return torch.softmax(x, dim=axis)
def _gelu(x):
    return torch.nn.functional.gelu(x, approximate="tanh")
def _silu(x):
    return torch.nn.functional.silu(x)
def _sigmoid(x):
    return torch.sigmoid(x)
def _layernorm(x, axis=-1, eps=1e-5):
    m = x.mean(dim=axis, keepdim=True)
    v = x.var(dim=axis, keepdim=True, unbiased=False)
    return (x - m) / torch.sqrt(v + eps)
def _scatter_add(tgt, idx, src, axis=0):
    return tgt.clone().index_add_(axis, idx, src)
def _segment_sum(src, seg, n):
    out = torch.zeros((n,) + src.shape[1:], dtype=src.dtype)
    return out.index_add_(0, seg, src)
def _pad(x, pw):
    flat = tuple(v for pair in reversed(pw) for v in pair)
    return torch.nn.functional.pad(x, flat)

kernel_name = 'gated_mix__sliding_local_dilated__dense_activation_gelu__prompt'

def setup_inputs():

    _w = (lambda a: (lambda t: t.to(torch.bfloat16) if t.is_floating_point() else t)(torch.as_tensor(a)))
    def _A_setup():
        B, S, D = 1, 2048, 640
        return {
            "x": _w((lambda *s: torch.randn(*s, dtype=torch.bfloat16) * 0.05)(B, S, D)),
            "wqkv": _w((lambda *s: torch.randn(*s, dtype=torch.bfloat16) * 0.8)(D, 3 * D)),
            "wo": _w((lambda *s: torch.randn(*s, dtype=torch.bfloat16) * 0.05)(D, D)),
        }
    _A_in = _A_setup()

    def _B_setup():
        B, S, D, F = 1, 2048, 640, 1536
        return {
            "x": _w((lambda *s: torch.randn(*s, dtype=torch.bfloat16) * 0.05)(B, S, D)),
            "w1": _w((lambda *s: torch.randn(*s, dtype=torch.bfloat16) * 12.8)(D, F) * (D ** -0.5)),
            "b1": _w((lambda *s: torch.randn(*s, dtype=torch.bfloat16) * 12.8)(F) * 0.01),
            "w2": _w((lambda *s: torch.randn(*s, dtype=torch.bfloat16) * 1.6)(F, D) * (F ** -0.5)),
            "b2": _w((lambda *s: torch.randn(*s, dtype=torch.bfloat16) * 12.8)(D) * 0.01),
        }
    _B_in = _B_setup()

    return {
        "A_x": _A_in["x"],
        "A_wqkv": _A_in["wqkv"],
        "A_wo": _A_in["wo"],
        "B_x": _B_in["x"],
        "B_w1": _B_in["w1"],
        "B_b1": _B_in["b1"],
        "B_w2": _B_in["w2"],
        "B_b2": _B_in["b2"],
        "gate_w": _w((lambda *s: torch.randn(*s, dtype=torch.bfloat16) * 0.05)(640) * 16.0),
    }

def reference(A_x, A_wqkv, A_wo, B_x, B_w1, B_b1, B_w2, B_b2, gate_w):
    def _A_ref(x, wqkv, wo):
        B, S, D = x.shape
        H = 32; Dh = D // H
        W, dil = 8, 2
        qkv = (lambda x, *d: x.permute(*d))((x @ wqkv).reshape(B, S, 3, H, Dh), 2, 0, 3, 1, 4)
        q, k, v = qkv[0], qkv[1], qkv[2]
        scale = Dh ** -0.5
        pos = torch.arange(S)
        off = torch.arange(W) * dil
        idx = pos[:, None] - off[None, :]
        mask = idx >= 0
        idx_c = torch.where(mask, idx, 0).reshape(-1)
        kg = k[:, :, idx_c, :].reshape(B, H, S, W, Dh)
        vg = v[:, :, idx_c, :].reshape(B, H, S, W, Dh)
        logits = torch.einsum("bhsd,bhswd->bhsw", q, kg) * scale
        logits = torch.where(mask[None, None, :, :], logits, -1e9)
        attn = _softmax(logits, axis=-1)
        o = torch.einsum("bhsw,bhswd->bhsd", attn, vg)
        return (lambda x, *d: x.permute(*d))(o, 0, 2, 1, 3).reshape(B, S, D) @ wo

    def _B_ref(x, w1, b1, w2, b2):
        h = torch.einsum("bsd,df->bsf", x, w1) + b1
        h = _gelu(h)
        return torch.einsum("bsf,fd->bsd", h, w2) + b2

    x = A_x
    g = _sigmoid((x * gate_w).sum(-1, keepdims=True))
    a = _A_ref(x, A_wqkv, A_wo)
    b = _B_ref(x, B_w1, B_b1, B_w2, B_b2)
    return g * a + (1 - g) * b
```

```cpp
#include <hip/hip_runtime.h>
#include <hip/hip_bf16.h>
typedef unsigned short u16;
__device__ __forceinline__ float bf2f(u16 v){ return __uint_as_float(((unsigned)v)<<16); }
__device__ __forceinline__ u16 f2bf(float f){ __hip_bfloat16 b = __float2bfloat16(f); return *(u16*)&b; }
__device__ __forceinline__ float rb(float f){ return bf2f(f2bf(f)); }
constexpr int S=2048,D=640,F=1536,H=32,DH=20;
__global__ void gemm(const u16* A,const u16* B,u16* C,int M,int N,int K,const u16* bias,int act){
  int n=blockIdx.x*64+threadIdx.x%64; int m=blockIdx.y*4+threadIdx.x/64;
  float acc=0; for(int k=0;k<K;++k) acc+=bf2f(A[m*K+k])*bf2f(B[k*N+n]);
  acc=rb(acc);
  if(bias) acc=rb(acc+bf2f(bias[n]));
  if(act){ float x=acc; float u=0.7978845608028654f*(x+0.044715f*x*x*x); acc=0.5f*x*(1.f+tanhf(u)); }
  C[m*N+n]=f2bf(acc);
}
__global__ void attn(const u16* qkv,u16* o){
  int idx=blockIdx.x*256+threadIdx.x; int s=idx/H,h=idx%H;
  float lg[8]; float mx=-1e30f;
  for(int w=0;w<8;++w){ int j=s-2*w; float a=0;
    if(j>=0){ for(int d=0;d<DH;++d) a+=bf2f(qkv[s*3*D+h*DH+d])*bf2f(qkv[j*3*D+D+h*DH+d]); a=rb(rb(a)*rb(0.22360679774997896f)); } else a=-1e9f;
    lg[w]=a; mx=fmaxf(mx,a);}
  float sum=0; for(int w=0;w<8;++w){ lg[w]=__expf(lg[w]-mx); sum+=lg[w]; }
  for(int w=0;w<8;++w) lg[w]=rb(lg[w]/sum);
  for(int d=0;d<DH;++d){ float a=0; for(int w=0;w<8;++w){ int j=s-2*w; if(j>=0) a+=lg[w]*bf2f(qkv[j*3*D+2*D+h*DH+d]); } o[s*D+h*DH+d]=f2bf(a);}
}
__global__ void fin(const u16* x,const u16* gw,const u16* a,const u16* b,u16* out){
  int s=blockIdx.x; __shared__ float red[64];
  float acc=0; for(int d=threadIdx.x;d<D;d+=64) acc+=rb(bf2f(x[s*D+d])*bf2f(gw[d]));
  red[threadIdx.x]=acc; __syncthreads();
  float t=0; for(int i=0;i<64;++i) t+=red[i];
  t=rb(t); float g=rb(1.f/(1.f+__expf(-t))); float og=rb(1.f-g);
  for(int d=threadIdx.x;d<D;d+=64) out[s*D+d]=f2bf(rb(g*bf2f(a[s*D+d]))+rb(og*bf2f(b[s*D+d])));
}
extern "C" void kernel_launch(void* const* d_in, const int* in_sizes, int n_in, void* d_out, int out_size, void* d_ws, size_t ws_size, hipStream_t stream) {
  const u16* x=(const u16*)d_in[0]; const u16* wqkv=(const u16*)d_in[1]; const u16* wo=(const u16*)d_in[2];
  const u16* w1=(const u16*)d_in[4]; const u16* b1=(const u16*)d_in[5]; const u16* w2=(const u16*)d_in[6]; const u16* b2=(const u16*)d_in[7]; const u16* gw=(const u16*)d_in[8];
  u16* ws=(u16*)d_ws; u16* qkv=ws; u16* o=qkv+S*3*D; u16* a=o+S*D; u16* h=a+S*D; u16* b=h+S*F;
  gemm<<<dim3(3*D/64,S/4),256,0,stream>>>(x,wqkv,qkv,S,3*D,D,nullptr,0);
  attn<<<S*H/256,256,0,stream>>>(qkv,o);
  gemm<<<dim3(D/64,S/4),256,0,stream>>>(o,wo,a,S,D,D,nullptr,0);
  gemm<<<dim3(F/64,S/4),256,0,stream>>>(x,w1,h,S,F,D,b1,1);
  gemm<<<dim3(D/64,S/4),256,0,stream>>>(h,w2,b,S,D,F,b2,0);
  fin<<<S,64,0,stream>>>(x,gw,a,b,(u16*)d_out);
}
```

```cpp
#include <hip/hip_runtime.h>
#include <hip/hip_bf16.h>
#include <stdint.h>
#include <type_traits>

namespace {

typedef unsigned short u16;
typedef unsigned v4u __attribute__((ext_vector_type(4)));
typedef unsigned v2u __attribute__((ext_vector_type(2)));
typedef float f32x4 __attribute__((ext_vector_type(4)));
typedef float f32x2 __attribute__((ext_vector_type(2)));
typedef __bf16 bf16x8 __attribute__((ext_vector_type(8)));
typedef __bf16 bf16x2 __attribute__((ext_vector_type(2)));
typedef short s16x4 __attribute__((ext_vector_type(4)));
#define LAS __attribute__((address_space(3)))
typedef LAS char* ldsp;

constexpr int D = 640, F = 1536;
constexpr unsigned WS_MB = 0;
constexpr unsigned WS_PQ = 65536;
constexpr unsigned WS_P2 = WS_PQ + 16u * 163840u;
constexpr unsigned WS_HA = WS_P2 + 8u * 348160u;
constexpr unsigned WS_END = WS_HA + 128u * 68u * 1024u;
constexpr int L_SLOTA = 28672;
constexpr int L_SLOTB = 16384;
constexpr int L_HALO = 114688;
constexpr int L_SCR = 135168;
constexpr int L_MISC = 159744;
constexpr int L_GW = L_MISC, L_B1 = L_MISC + 1280, L_B2 = L_MISC + 1472;
constexpr int L_G = L_MISC + 2048;
constexpr int L_CTL = L_MISC + 3072;
constexpr int L_KV = 124288;
constexpr int L_Q = L_KV + 144 * 168;
constexpr int L_RING2 = 15360;
constexpr int LDS_BYTES = 163840;
static_assert(L_Q + 128 * 88 <= L_MISC, "lds");
static_assert(8 * L_RING2 <= L_KV, "lds");
constexpr int CTRL = 7;
constexpr unsigned SPIN_CAP = 1u << 18;

template <int I, int N, class Fn>
__device__ __forceinline__ void sfor(Fn&& f) {
  if constexpr (I < N) {
    f(std::integral_constant<int, I>{});
    sfor<I + 1, N>(f);
  }
}

template <int OFF, bool SC1>
__device__ __forceinline__ void dma_s(unsigned voff, const void* sbase, unsigned ldsaddr) {
  if constexpr (SC1)
    asm volatile("s_mov_b32 m0, %2\n\ts_nop 0\n\tglobal_load_lds_dwordx4 %0, %1 offset:%3 sc1" ::"v"(voff), "s"(sbase), "s"(ldsaddr), "n"(OFF) : "memory");
  else
    asm volatile("s_mov_b32 m0, %2\n\ts_nop 0\n\tglobal_load_lds_dwordx4 %0, %1 offset:%3" ::"v"(voff), "s"(sbase), "s"(ldsaddr), "n"(OFF) : "memory");
}
__device__ __forceinline__ void dma_s2(unsigned voff, const void* sbase, unsigned ldsaddr) {
  asm volatile("s_mov_b32 m0, %2\n\ts_nop 0\n\tglobal_load_lds_dwordx4 %0, %1 sc1\n\tglobal_load_lds_dwordx4 %0, %1 offset:1024 sc1" ::"v"(voff), "s"(sbase), "s"(ldsaddr) : "memory");
}
__device__ __forceinline__ void dma_s5(unsigned voff, const void* sbase_p2048, unsigned ldsaddr_p2048) {
  asm volatile(
      "s_mov_b32 m0, %2\n\ts_nop 0\n\t"
      "global_load_lds_dwordx4 %0, %1 offset:-2048 sc1\n\t"
      "global_load_lds_dwordx4 %0, %1 offset:-1024 sc1\n\t"
      "global_load_lds_dwordx4 %0, %1 sc1\n\t"
      "global_load_lds_dwordx4 %0, %1 offset:1024 sc1\n\t"
      "global_load_lds_dwordx4 %0, %1 offset:2048 sc1" ::"v"(voff), "s"(sbase_p2048), "s"(ldsaddr_p2048) : "memory");
}
__device__ __forceinline__ void dma_v(const void* vptr, unsigned ldsaddr) {
  asm volatile("s_mov_b32 m0, %1\n\ts_nop 0\n\tglobal_load_lds_dwordx4 %0, off" ::"v"(vptr), "s"(ldsaddr) : "memory");
}
template <int OFF>
__device__ __forceinline__ void st16_sc1(unsigned voff, const void* sbase, v4u d) {
  asm volatile("global_store_dwordx4 %0, %1, %2 offset:%3 sc1\n\ts_nop 1" ::"v"(voff), "v"(d), "s"(sbase), "n"(OFF) : "memory");
}
__device__ __forceinline__ void st4_sc1(unsigned voff, const void* sbase, unsigned d) {
  asm volatile("global_store_dword %0, %1, %2 sc1" ::"v"(voff), "v"(d), "s"(sbase) : "memory");
}
template <int OFF>
__device__ __forceinline__ void ld16_sc1(v4u& d, unsigned voff, const void* sbase) {
  asm volatile("global_load_dwordx4 %0, %1, %2 offset:%3 sc1" : "=v"(d) : "v"(voff), "s"(sbase), "n"(OFF) : "memory");
}
__device__ __forceinline__ unsigned ld4_sc1_wait(unsigned voff, const void* sbase) {
  unsigned d;
  asm volatile("global_load_dword %0, %1, %2 sc1\n\ts_waitcnt vmcnt(0)" : "=&v"(d) : "v"(voff), "s"(sbase) : "memory");
  return d;
}
template <int N>
__device__ __forceinline__ void wait_bar() {
  static_assert(N >= 0 && N <= 63, "vmcnt");
  asm volatile("s_waitcnt vmcnt(%0) lgkmcnt(0)\n\ts_barrier" ::"n"(N) : "memory");
}
__device__ __forceinline__ void lds_bar() { asm volatile("s_waitcnt lgkmcnt(0)\n\ts_barrier" ::: "memory"); }
template <int N>
__device__ __forceinline__ void wait_vm() {
  static_assert(N >= 0 && N <= 63, "vmcnt");
  asm volatile("s_waitcnt vmcnt(%0)" ::"n"(N) : "memory");
}
__device__ __forceinline__ void pin4(v4u& a, v4u& b, v4u& c, v4u& d) { asm volatile("" : "+v"(a), "+v"(b), "+v"(c), "+v"(d)); }

__device__ __forceinline__ v4u lds16(ldsp p, int off) { return *(const LAS v4u*)(p + off); }
__device__ __forceinline__ v2u lds8(ldsp p, int off) { return *(const LAS v2u*)(p + off); }
__device__ __forceinline__ v2u ldstr(ldsp p, int off) {
  return __builtin_bit_cast(v2u, __builtin_amdgcn_ds_read_tr16_b64_v4i16((LAS s16x4*)(p + off)));
}
__device__ __forceinline__ f32x4 mfma(v4u a, v4u b, f32x4 c) {
  return __builtin_amdgcn_mfma_f32_16x16x32_bf16(__builtin_bit_cast(bf16x8, a), __builtin_bit_cast(bf16x8, b), c, 0, 0, 0);
}
__device__ __forceinline__ unsigned pk2(float a, float b) {
  f32x2 v = {a, b};
  return __builtin_bit_cast(unsigned, __builtin_convertvector(v, bf16x2));
}
__device__ __forceinline__ float bflo(unsigned u) { return __uint_as_float(u << 16); }
__device__ __forceinline__ float bfhi(unsigned u) { return __uint_as_float(u & 0xffff0000u); }
__device__ __forceinline__ float rbf(float a) { return bflo(pk2(a, a)); }
__device__ __forceinline__ float dot2(unsigned a, unsigned b, float c) {
  return __builtin_amdgcn_fdot2_f32_bf16(__builtin_bit_cast(bf16x2, a), __builtin_bit_cast(bf16x2, b), c, false);
}
__device__ __forceinline__ float xor1(float v) {
  return __int_as_float(__builtin_amdgcn_update_dpp(0, __float_as_int(v), 0xB1, 0xF, 0xF, true));
}
__device__ __forceinline__ unsigned gelu2(f32x2 x, float s) {
  const f32x2 c0 = {-2.3022081983f, -2.3022081983f}, c1 = {-0.1029432397f, -0.1029432397f};
  f32x2 z = x * (x * x * c1 + c0);
  f32x2 e = {__builtin_amdgcn_exp2f(z[0]), __builtin_amdgcn_exp2f(z[1])};
  e = e + 1.0f;
  f32x2 r = {__builtin_amdgcn_rcpf(e[0]), __builtin_amdgcn_rcpf(e[1])};
  f32x2 y = (x * s) * r;
  return pk2(y[0], y[1]);
}

constexpr int JH = 4;
constexpr int JP = 4;
constexpr int halo_n(int i) { return i == 6 ? 2 : (i >= 7 && i <= 9) ? 1 : 0; }
constexpr int halo_first(int i) { return i == 6 ? 0 : i - 5; }
struct Sched {
  int wa[10], wb[10];
};
constexpr Sched make_sched(int hf) {
  Sched s{};
  int n = 0, lastA[10] = {}, lastB[10] = {}, lastHalo[5] = {}, lastH = 0;
  const int nA = hf == 0 ? 4 : 3;
  n += 3 + 1;
  for (int i = 0; i < 4; ++i) { n += nA; lastA[i] = n; }
  for (int i = 0; i < 10; ++i) {
    s.wa[i] = n - lastA[i];
    if (i == 0) n += 3;
    if (i >= 1 && i <= 6) { n += nA; lastA[i + 3] = n; }
    if (hf == 1) for (int k = 0; k < halo_n(i); ++k) { n += 1; lastHalo[halo_first(i) + k] = n; }
    if (i == 7) { n += 2; lastB[0] = n; }
    if (i == 8) { n += 2; lastB[1] = n; n += 2; lastB[2] = n; }
    if (i == 9) { n += 2; lastB[3] = n; }
  }
  for (int j = 4; j <= 6; ++j) { n += 2; lastB[j] = n; }
  n += hf == 0 ? 4 : 2;
  lastH = n;
  for (int j = 0; j < 10; ++j) {
    int need = lastB[j];
    if (hf == 1 && lastHalo[j >> 1] > need) need = lastHalo[j >> 1];
    if (j == JH && lastH > need) need = lastH;
    s.wb[j] = n - need;
    if (j >= 1 && j <= 3) { n += 2; lastB[j + 6] = n; }
  }
  return s;
}

struct Params {
  const u16* x;
  const u16* wqkv;
  const u16* wo;
  const u16* w1;
  const u16* b1;
  const u16* w2;
  const u16* b2;
  const u16* gw;
  u16* out;
  char* ws;
};

#define STAMP(i)

__device__ __forceinline__ void poll_mail(const char* ws, unsigned mb_off, bool mine, unsigned lvl, unsigned ack_off, bool ack_on,
                                          unsigned ack_val) {
  for (unsigned spin = 0; spin < SPIN_CAP; ++spin) {
    unsigned v = ld4_sc1_wait(mb_off, ws);
    bool ok = !mine || (v >= lvl && v <= 2u);
    if (__all(ok)) break;
    if (ack_on) st4_sc1(ack_off, ws, ack_val);
    __builtin_amdgcn_s_sleep(2);
  }
}

template <int HF>
__device__ __forceinline__ void stage1(const Params& p, ldsp lds, const int w, const int lane, const int rb, const int cg) {
  constexpr Sched SC = make_sched(HF);
  const int s = w & 3, r16 = lane & 15, g = lane >> 4;
  const int ct = cg & 7, hf2 = cg >> 3;
  const char* ws = p.ws;
  const unsigned ldsb = (unsigned)(size_t)lds;
  const unsigned myL = rb * 16 + cg;
  const unsigned mb_off = WS_MB + myL * 256 + (lane < 48 ? lane : 0) * 4;
  const unsigned ack_off = lane < 32 ? WS_MB + ((lane >> 1) * 16 + ct + 8 * (lane & 1)) * 256 + (rb * 2 + hf2) * 4
                                     : WS_MB + (rb * 16 + ((lane - 32) & 15)) * 256 + (32 + cg) * 4;
  if (HF == 1 && w == CTRL) {
    if (lane < 48) st4_sc1(mb_off, ws, 0u);
    if (lane < 8) *(LAS unsigned*)(lds + L_CTL + lane * 4) = 0u;
  }
  const int jme = rb * 2 + hf2;
  const int gbase = jme < 16 ? 9 * jme : 144 + 8 * (jme - 16), gcnt = jme < 16 ? 9 : 8;
  sfor<0, 3>([&](auto I) {
    const int Q = (3 * w + I) * 64 + lane;
    const u16* src;
    if (Q < 600) {
      const int gi = Q / 120, rem = Q % 120, row = rem / 15, ch = rem % 15, seg = ch / 5, c5 = ch % 5;
      src = p.wqkv + ((5 * rb + gi) * 8 + row) * 1920 + (seg == 0 ? 640 : seg == 1 ? 1280 : 0) + cg * 40 + c5 * 8;
    } else {
      const int Qp = Q < 1320 ? Q - 600 : 0;
      int gi = Qp / 80;
      const int rem = Qp % 80, row = rem / 10, ch = rem % 10;
      gi = gi < gcnt ? gi : gcnt - 1;
      const int G = gbase + gi, t = G >> 2, gg = G & 3;
      src = (t < 48 ? p.w2 + (32 * t + 16 * (row >> 2) + 4 * gg + (row & 3)) * 640 : p.wo + (32 * (t - 48) + 8 * gg + row) * 640) + ct * 80 + ch * 8;
    }
    dma_v(src, ldsb + L_SCR + (3 * w + I) * 1024);
  });
  {
    const int c = 16 * w + r16;
    const u16* src = c < 80 ? p.gw + c * 8 : c < 92 ? p.b1 + cg * 96 + (c - 80) * 8 : c < 102 ? p.b2 + ct * 80 + (c - 92) * 8 : p.gw;
    if (lane < 16) dma_v(src, ldsb + L_MISC + w * 256);
  }
  const unsigned xo0 = (lane >> 3) * 1280 + (((lane & 7) ^ (lane >> 4)) * 16);
  const unsigned xo1 = (8 + (lane >> 3)) * 1280 + (((lane & 7) ^ (4 + (lane >> 4))) * 16);
  const char* xb = (const char*)p.x + (size_t)(rb * 128 + 16 * w) * 1280;
  unsigned wo_[2];
  sfor<0, 2 - HF>([&](auto I) {
    const int P = HF == 0 ? s + 4 * I : 8 + s;
    const int Q = 64 * P + lane, rowp = Q / 12, pos = Q % 12;
    const int c = (pos >> 1) ^ ((rowp >> 2) & 1);
    const int r = (rowp & 0x33) | ((rowp & 4) << 1) | ((rowp & 8) >> 1);
    wo_[I] = r * 3072 + c * 32 + (pos & 1) * 16;
  });
  const char* w1b = (const char*)p.w1 + cg * 192;
  auto issueA = [&](int st) {
    const unsigned sl = ldsb + (st & 3) * L_SLOTA;
    dma_s<0, false>(xo0, xb + st * 128, sl + w * 2048);
    dma_s<0, false>(xo1, xb + st * 128, sl + w * 2048 + 1024);
    if constexpr (HF == 0) {
      dma_s<0, false>(wo_[0], w1b + st * 196608, sl + 16384 + s * 1024);
      dma_s<0, false>(wo_[1], w1b + st * 196608, sl + 16384 + (s + 4) * 1024);
    } else {
      dma_s<0, false>(wo_[0], w1b + st * 196608, sl + 16384 + (8 + s) * 1024);
    }
  };
  const char* pqb = ws + WS_PQ + cg * 163840 + w * 2048;
  auto issueB = [&](int st) { dma_s2(lane * 16, pqb + st * 16384, ldsb + ((st + 4) % 7) * L_SLOTB + w * 2048); };
  issueA(0); issueA(1); issueA(2); issueA(3);
  STAMP(0);

  const int xa = (32 * s + r16) * 128 + ((g ^ (r16 >> 1)) * 16);
  const int q4 = r16 >> 2, p4 = lane & 3;
  const int wa = 16384 + 192 * (16 * (g >> 1) + 4 * (g & 1) + q4) + 8 * p4;
  const int wae = wa + 32 * (g & 1), wao = wa - 32 * (g & 1);

  v4u xf[2][20];
  constexpr int NA = HF == 0 ? 4 : 2;
  f32x4 acc[2][NA];
  f32x4 accg[2];
  sfor<0, 2>([&](auto M) {
    sfor<0, NA>([&](auto N) { acc[M][N] = f32x4{0, 0, 0, 0}; });
    accg[M] = f32x4{0, 0, 0, 0};
  });

  sfor<0, 10>([&](auto I) {
    constexpr int i = I;
    wait_bar<SC.wa[i]>();
    if constexpr (i == 0) {
      STAMP(1);
      sfor<0, 3>([&](auto J) {
        int U = (3 * w + J) * 4 + g;
        U = U < 85 ? U : U - 11;
        int la;
        unsigned dst;
        if (U < 40) {
          const int gi = U >> 3, nt = U & 7, Gq = 5 * rb + gi;
          la = L_SCR + 1920 * gi + q4 * 240 + nt * 32 + p4 * 8;
          dst = WS_PQ + cg * 163840 + (Gq >> 2) * 8192 + nt * 1024 + ((Gq & 3) * 16 + r16) * 16;
        } else {
          const int Up = U - 40, gi = Up / 5, nt = Up % 5, G = gbase + (gi < gcnt ? gi : gcnt - 1);
          la = L_SCR + 9600 + 1280 * gi + q4 * 160 + nt * 32 + p4 * 8;
          dst = WS_P2 + ct * 348160 + (G >> 2) * 5120 + nt * 1024 + ((G & 3) * 16 + r16) * 16;
        }
        const int pitch4 = U < 40 ? 960 : 640;
        const v2u lo = ldstr(lds, la), hi = ldstr(lds, la + pitch4);
        st16_sc1<0>(dst, ws, v4u{lo[0], lo[1], hi[0], hi[1]});
      });
    }
    if constexpr (i == JP && HF == 1) {
      if (w == CTRL && lane < 32) st4_sc1(ack_off, ws, 1u);
    }
    constexpr int sl = (i & 3) * L_SLOTA;
    sfor<0, 2>([&](auto KS) {
      constexpr int ks = KS, kk = 2 * i + ks;
      xf[0][kk] = lds16(lds, sl + (xa ^ (ks * 64)));
      xf[1][kk] = lds16(lds, sl + 2048 + (xa ^ (ks * 64)));
      v4u bf[NA];
      sfor<0, NA>([&](auto N) {
        constexpr int c = HF == 0 ? (int)N : 4 + N;
        const int a0 = sl + ((c & 1) ? wao : wae) + 192 * 32 * ks + 32 * c;
        const v2u lo = ldstr(lds, a0), hi = ldstr(lds, a0 + 192 * 8);
        bf[N] = v4u{lo[0], lo[1], hi[0], hi[1]};
      });
      v4u gf;
      if constexpr (HF == 1) gf = lds16(lds, L_GW + kk * 64 + g * 16);
      if constexpr (ks == 0) {
        if constexpr (i >= 1 && i <= 6) issueA(i + 3);
        if constexpr (HF == 1 && halo_n(i) > 0) {
          const int hrow = rb * 128 - 16 + 8 * (s & 1) + (lane >> 3);
          const unsigned ho = (hrow > 0 ? hrow : 0) * 1280 + (((lane & 7) ^ (4 * (s & 1) + (lane >> 4))) * 16);
          sfor<0, halo_n(i)>([&](auto K) {
            const int st = 2 * (halo_first(i) + K) + (s >> 1);
            dma_s<0, false>(ho, (const char*)p.x + st * 128, ldsb + L_HALO + st * 2048 + (s & 1) * 1024);
          });
        }
        if constexpr (i == 7) issueB(0);
        if constexpr (i == 8) { issueB(1); issueB(2); }
        if constexpr (i == 9) issueB(3);
      }
      sfor<0, NA>([&](auto N) {
        acc[0][N] = mfma(bf[N], xf[0][kk], acc[0][N]);
        acc[1][N] = mfma(bf[N], xf[1][kk], acc[1][N]);
      });
      if constexpr (HF == 1) {
        accg[0] = mfma(gf, xf[0][kk], accg[0]);
        accg[1] = mfma(gf, xf[1][kk], accg[1]);
      }
    });
    if constexpr (i == 6 && HF == 1) {
      if (w == CTRL) {
        STAMP(2);
        poll_mail(ws, mb_off, lane < 32, 1u, ack_off, lane < 32, 1u);
        if (lane < 32) st4_sc1(ack_off, ws, 1u);
        STAMP(3);
      }
    }
  });
  if constexpr (HF == 1) {
    sfor<0, 2>([&](auto M) {
      const float t = rbf(accg[M][0]);
      const float gg = rbf(__builtin_amdgcn_rcpf(1.0f + __builtin_amdgcn_exp2f(-1.4426950409f * t)));
      const float og = rbf(1.0f - gg);
      if (g == 0) *(LAS f32x2*)(lds + L_G + (32 * s + 16 * M + r16) * 8) = f32x2{gg, og};
    });
  }
  lds_bar();
  STAMP(4);
  issueB(4); issueB(5); issueB(6);
  {
    const char* hab = ws + WS_HA + (size_t)((8 * rb + 2 * s) * 68 + 3 * cg + 2 * HF) * 1024;
    sfor<0, 2>([&](auto M) {
      const float og = *(const LAS float*)(lds + L_G + (32 * s + 16 * M + r16) * 8 + 4);
      sfor<0, NA / 2>([&](auto T) {
        v4u o;
        sfor<0, 2>([&](auto Hh) {
          constexpr int n = 2 * T + Hh, c = (HF == 0 ? 0 : 4) + n;
          const v2u bb = lds8(lds, L_B1 + c * 32 + g * 8);
          const f32x4 a = acc[M][n];
          o[2 * Hh] = gelu2(f32x2{a[0] + bflo(bb[0]), a[1] + bfhi(bb[0])}, og);
          o[2 * Hh + 1] = gelu2(f32x2{a[2] + bflo(bb[1]), a[3] + bfhi(bb[1])}, og);
        });
        st16_sc1<T * 1024>(lane * 16, hab + M * 69632, o);
      });
    });
  }
  STAMP(5);
  constexpr int NB = HF == 0 ? 3 : 5;
  f32x4 ac2[2][NB];
  f32x4 ach[2];
  sfor<0, 2>([&](auto M) {
    sfor<0, NB>([&](auto N) { ac2[M][N] = f32x4{0, 0, 0, 0}; });
    ach[M] = f32x4{0, 0, 0, 0};
  });
  const int ha = L_HALO + r16 * 128 + ((g ^ (r16 >> 1)) * 16);
  const int l16 = lane * 16, l16s = lane * 16 + s * 1024;
  sfor<0, 10>([&](auto J) {
    constexpr int j = J;
    wait_bar<SC.wb[j]>();
    if constexpr (j == JH && HF == 1) {
      if (w == CTRL && lane >= 32 && lane < 48) st4_sc1(ack_off, ws, 1u);
    }
    constexpr int sl = ((j + 4) % 7) * L_SLOTB;
    sfor<0, 2>([&](auto KS) {
      constexpr int ks = KS, kk = 2 * j + ks;
      v4u bf[NB];
      sfor<0, NB>([&](auto N) { bf[N] = lds16(lds, sl + ks * 8192 + ((HF == 0 ? 5 : 0) + N) * 1024 + l16); });
      v4u hx, hb0, hb1;
      if constexpr (HF == 0) {
        hx = lds16(lds, j * 2048 + (ha ^ (ks * 64)));
        hb0 = lds16(lds, sl + ks * 8192 + l16s);
        hb1 = lds16(lds, sl + ks * 8192 + 4096 + l16);
      }
      if constexpr (ks == 0 && j >= 1 && j <= 3) issueB(j + 6);
      sfor<0, NB>([&](auto N) {
        ac2[0][N] = mfma(bf[N], xf[0][kk], ac2[0][N]);
        ac2[1][N] = mfma(bf[N], xf[1][kk], ac2[1][N]);
      });
      if constexpr (HF == 0) {
        ach[0] = mfma(hb0, hx, ach[0]);
        ach[1] = mfma(hb1, hx, ach[1]);
      }
    });
  });
  STAMP(6);
  lds_bar();
  auto put = [&](int off, f32x4 a) { *(LAS v2u*)(lds + off) = v2u{pk2(a[0], a[1]), pk2(a[2], a[3])}; };
  if constexpr (HF == 1) {
    sfor<0, 2>([&](auto M) {
      sfor<0, 5>([&](auto N) { put(L_KV + (16 + 32 * s + 16 * M + r16) * 168 + 32 * N + 8 * g, ac2[M][N]); });
    });
  } else {
    sfor<0, 2>([&](auto M) {
      sfor<0, 2>([&](auto N) { put(L_Q + (32 * s + 16 * M + r16) * 88 + 32 * N + 8 * g, ac2[M][N]); });
      if (g < 2) put(L_Q + (32 * s + 16 * M + r16) * 88 + 64 + 8 * g, ac2[M][2]);
    });
    put(L_KV + r16 * 168 + 32 * s + 8 * g, ach[0]);
    if (s == 0) put(L_KV + r16 * 168 + 128 + 8 * g, ach[1]);
  }
}

template <bool OW>
__device__ __forceinline__ void stage2(const Params& p, ldsp lds, const int w, const int lane, const int rb, const int cg) {
  const int r16 = lane & 15, g = lane >> 4;
  const int ct = cg & 7, hf2 = cg >> 3;
  const char* ws = p.ws;
  const unsigned ldsb = (unsigned)(size_t)lds;
  constexpr int NK = OW ? 9 : 8;
  constexpr int NA0 = OW ? 4 : 6;
  const int k0 = OW ? 4 * w : 16 + 8 * (w - 4), k1 = 48 + 5 * w - 4;
  const unsigned l16 = lane * 16;
  const char* bb = ws + WS_P2 + ct * 348160 + 2048;
  const unsigned ring = ldsb + w * L_RING2 + 2048;
  auto issueB = [&](auto I) {
    constexpr int i = I;
    const int kk = ((OW && i >= 4) ? k1 : k0) + i;
    dma_s5(l16, bb + kk * 5120, ring + (i % 3) * 5120);
  };
  const char* ab = ws + WS_HA + (size_t)(8 * rb + 4 * hf2) * 69632;
  v4u af[NK][4];
  auto issueA = [&](auto I) {
    constexpr int i = I;
    const int kk = ((OW && i >= 4) ? k1 : k0) + i;
    sfor<0, 4>([&](auto M) { ld16_sc1<0>(af[i][M], l16, ab + M * 69632 + kk * 1024); });
  };
  const unsigned myL = rb * 16 + cg;
  const unsigned mb_off = WS_MB + myL * 256 + (32 + r16) * 4;
  const unsigned ack_off = WS_MB + (rb * 16 + r16) * 256 + (32 + cg) * 4;

  sfor<0, 3>(issueB);
  if (!OW && w == CTRL) {
    STAMP(7);
    poll_mail(ws, mb_off, true, 1u, ack_off, lane < 16, 1u);
    if (lane < 16) st4_sc1(ack_off, ws, 1u);
    STAMP(8);
  }
  lds_bar();
  sfor<0, NA0>(issueA);
  STAMP(9);
  {
    const int row = 16 * w + (lane >> 2), hh = (lane >> 1) & 1, wh = lane & 1;
    const int qa = L_Q + row * 88 + hh * 40;
    const int ka = L_KV + (16 + row - 8 * wh) * 168 + hh * 40;
    v2u q[5];
    sfor<0, 5>([&](auto C) { q[C] = lds8(lds, qa + C * 8); });
    float lg[4];
    sfor<0, 4>([&](auto I) {
      float a = 0.f;
      sfor<0, 5>([&](auto C) {
        const v2u k = lds8(lds, ka - I * 336 + C * 8);
        a = dot2(q[C][0], k[0], a);
        a = dot2(q[C][1], k[1], a);
      });
      a *= 0.22360679774997896f * 1.4426950408889634f;
      lg[I] = (rb * 128 + row - 8 * wh - 2 * I >= 0) ? a : -1e9f * 1.4426950408889634f;
    });
    float mx = fmaxf(fmaxf(lg[0], lg[1]), fmaxf(lg[2], lg[3]));
    mx = fmaxf(mx, xor1(mx));
    float e[4], sum = 0.f;
    sfor<0, 4>([&](auto I) { e[I] = __builtin_amdgcn_exp2f(lg[I] - mx); sum += e[I]; });
    sum += xor1(sum);
    const float inv = __builtin_amdgcn_rcpf(sum);
    const unsigned p01 = pk2(e[0] * inv, e[1] * inv), p23 = pk2(e[2] * inv, e[3] * inv);
    const float gg = *(const LAS float*)(lds + L_G + row * 8);
    unsigned ov[10];
    sfor<0, 5>([&](auto C) {
      v2u v[4];
      sfor<0, 4>([&](auto I) { v[I] = lds8(lds, ka + 80 - I * 336 + C * 8); });
      sfor<0, 2>([&](auto E_) {
        constexpr int E = E_;
        float lo = dot2(__builtin_amdgcn_perm(v[1][E], v[0][E], 0x05040100u), p01, 0.f);
        float hi = dot2(__builtin_amdgcn_perm(v[1][E], v[0][E], 0x07060302u), p01, 0.f);
        lo = dot2(__builtin_amdgcn_perm(v[3][E], v[2][E], 0x05040100u), p23, lo);
        hi = dot2(__builtin_amdgcn_perm(v[3][E], v[2][E], 0x07060302u), p23, hi);
        lo += xor1(lo);
        hi += xor1(hi);
        ov[2 * C + E] = pk2(lo * gg, hi * gg);
      });
    });
    const int oa = qa + wh * 24;
    *(LAS v2u*)(lds + oa) = wh ? v2u{ov[6], ov[7]} : v2u{ov[0], ov[1]};
    *(LAS v2u*)(lds + oa + 8) = wh ? v2u{ov[8], ov[9]} : v2u{ov[2], ov[3]};
    if (!wh) *(LAS v2u*)(lds + oa + 16) = v2u{ov[4], ov[5]};
    const char* ob = ws + WS_HA + (size_t)((8 * rb + w) * 68 + 48) * 1024 + cg * 1280;
    asm volatile("s_waitcnt lgkmcnt(0)" ::: "memory");
    sfor<0, 2>([&](auto I) {
      const int c = I * 40 + (lane < 40 ? lane : 0);
      const int la = L_Q + (16 * w + (c & 15)) * 88 + (c >> 4) * 16;
      const v2u a = lds8(lds, la), b = lds8(lds, la + 8);
      if (lane < 40) st16_sc1<0>(c * 16, ob, v4u{a[0], a[1], b[0], b[1]});
    });
  }
  STAMP(10);
  f32x4 acc[4][5];
  sfor<0, 4>([&](auto M) { sfor<0, 5>([&](auto N) { acc[M][N] = f32x4{0, 0, 0, 0}; }); });
  constexpr int JO = 1;
  struct S2 { int wv[9]; };
  constexpr S2 s2 = [] {
    S2 r{};
    int n = 0, lastB[12] = {}, lastA[12] = {};
    for (int i = 0; i < 3; ++i) { n += 5; lastB[i] = n; }
    for (int i = 0; i < NA0; ++i) { n += 4; lastA[i] = n; }
    n += 2;
    const int lastO = n;
    for (int i = 0; i < NK; ++i) {
      if (OW && i == 4) for (int k = 4; k < 9; ++k) { n += 4; lastA[k] = n; }
      int need = lastB[i] > lastA[i] ? lastB[i] : lastA[i];
      if (i == JO && lastO > need) need = lastO;
      r.wv[i] = n - need;
      if (i + 3 < NK) { n += 5; lastB[i + 3] = n; }
      if (!OW && i + 6 < NK) { n += 4; lastA[i + 6] = n; }
    }
    return r;
  }();
  const int rg = w * L_RING2 + l16;
  sfor<0, NK>([&](auto I) {
    constexpr int i = I;
    if constexpr (OW && i == 4) {
      STAMP(11);
      for (unsigned spin = 0; spin < SPIN_CAP * 16; ++spin) {
        const unsigned f = *(volatile LAS unsigned*)(lds + L_CTL + 16 + w * 4);
        if (__builtin_amdgcn_readfirstlane(f) != 0u) break;
        __builtin_amdgcn_s_sleep(1);
      }
      asm volatile("" ::: "memory");
      sfor<0, 20>([&](auto T) {
        constexpr int t = T;
        const int a = t < 15 ? (w + 4) * L_RING2 + t * 1024 + l16 : L_KV + w * 5120 + (t - 15) * 1024 + l16;
        acc[t / 5][t % 5] += __builtin_bit_cast(f32x4, lds16(lds, a));
      });
      STAMP(12);
      poll_mail(ws, mb_off, true, 2u, ack_off, false, 0u);
      STAMP(13);
      sfor<4, 9>(issueA);
    }
    wait_vm<s2.wv[i]>();
    pin4(af[i][0], af[i][1], af[i][2], af[i][3]);
    if constexpr (i == JO) {
      unsigned old = 0;
      if (lane == 0) old = __hip_atomic_fetch_add((LAS unsigned*)(lds + L_CTL), 1u, __ATOMIC_RELAXED, __HIP_MEMORY_SCOPE_WORKGROUP);
      if (__builtin_amdgcn_readfirstlane(old) == 7u) {
        if (lane < 16) st4_sc1(ack_off, ws, 2u);
      }
    }
    v4u bf[5];
    sfor<0, 5>([&](auto N) { bf[N] = lds16(lds, rg + (i % 3) * 5120 + N * 1024); });
    if constexpr (i + 3 < NK) {
      asm volatile("s_waitcnt lgkmcnt(0)" : "+v"(bf[0]), "+v"(bf[1]), "+v"(bf[2]), "+v"(bf[3]), "+v"(bf[4])::"memory");
      issueB(std::integral_constant<int, i + 3>{});
    }
    sfor<0, 4>([&](auto M) { sfor<0, 5>([&](auto N) { acc[M][N] = mfma(bf[N], af[i][M], acc[M][N]); }); });
    if constexpr (!OW && i + 6 < NK) issueA(std::integral_constant<int, i + 6>{});
  });
  STAMP(14);
  if constexpr (!OW) {
    for (unsigned spin = 0; spin < SPIN_CAP * 16; ++spin) {
      const unsigned f = *(volatile LAS unsigned*)(lds + L_CTL);
      if (__builtin_amdgcn_readfirstlane(f) >= 8u) break;
      __builtin_amdgcn_s_sleep(1);
    }
    asm volatile("" ::: "memory");
    sfor<0, 20>([&](auto T) {
      constexpr int t = T;
      const int a = t < 15 ? w * L_RING2 + t * 1024 + l16 : L_KV + (w - 4) * 5120 + (t - 15) * 1024 + l16;
      *(LAS f32x4*)(lds + a) = acc[t / 5][t % 5];
    });
    asm volatile("s_waitcnt lgkmcnt(0)" ::: "memory");
    if (lane == 0) *(volatile LAS unsigned*)(lds + L_CTL + 16 + (w - 4) * 4) = 1u;
    lds_bar();
  } else {
    sfor<0, 4>([&](auto M) {
      constexpr int m = M;
      if (m != w) {
        const int slot = m < w ? m : m - 1;
        sfor<0, 5>([&](auto N) { *(LAS f32x4*)(lds + rg + (slot * 5 + N) * 1024) = acc[m][N]; });
      }
    });
    f32x4 own[5];
    sfor<0, 5>([&](auto N) { own[N] = w == 0 ? acc[0][N] : w == 1 ? acc[1][N] : w == 2 ? acc[2][N] : acc[3][N]; });
    lds_bar();
    sfor<1, 4>([&](auto Dd) {
      const int o = (w + Dd) & 3;
      const int slot = w < o ? w : w - 1;
      sfor<0, 5>([&](auto N) { own[N] += __builtin_bit_cast(f32x4, lds16(lds, o * L_RING2 + (slot * 5 + N) * 1024 + l16)); });
    });
    const int row = 64 * hf2 + 16 * w + r16;
    const float og = *(const LAS float*)(lds + L_G + row * 8 + 4);
    u16* op = p.out + (size_t)(rb * 128 + row) * 640 + ct * 80 + g * 4;
    sfor<0, 5>([&](auto N) {
      const v2u b = lds8(lds, L_B2 + N * 32 + g * 8);
      const f32x4 a = own[N];
      *(v2u*)(op + N * 16) = v2u{pk2(a[0] + og * bflo(b[0]), a[1] + og * bfhi(b[0])), pk2(a[2] + og * bflo(b[1]), a[3] + og * bfhi(b[1]))};
    });
  }
  STAMP(15);
}

__global__ __launch_bounds__(512, 2) void fused_kernel(Params p) {
  extern __shared__ __attribute__((aligned(16))) char smem[];
  ldsp lds = (ldsp)smem;
  const int tid = threadIdx.x, lane = tid & 63;
  const int w = __builtin_amdgcn_readfirstlane(tid >> 6);
  const int b = blockIdx.x, xcd = b & 7, idx = b >> 3;
  const int rb = (xcd >> 2) * 8 + (idx & 7), cg = (xcd & 3) * 4 + (idx >> 3);
  if (w < 4) {
    stage1<0>(p, lds, w, lane, rb, cg);
    stage2<true>(p, lds, w, lane, rb, cg);
  } else {
    stage1<1>(p, lds, w, lane, rb, cg);
    stage2<false>(p, lds, w, lane, rb, cg);
  }
}

}

extern "C" void kernel_launch(void* const* d_in, const int* in_sizes, int n_in, void* d_out, int out_size, void* d_ws, size_t ws_size,
                              hipStream_t stream) {
  Params p;
  p.x = (const u16*)d_in[0];
  p.wqkv = (const u16*)d_in[1];
  p.wo = (const u16*)d_in[2];
  p.w1 = (const u16*)d_in[4];
  p.b1 = (const u16*)d_in[5];
  p.w2 = (const u16*)d_in[6];
  p.b2 = (const u16*)d_in[7];
  p.gw = (const u16*)d_in[8];
  p.out = (u16*)d_out;
  p.ws = (char*)d_ws;
  (void)hipFuncSetAttribute((const void*)fused_kernel, hipFuncAttributeMaxDynamicSharedMemorySize, LDS_BYTES);
  hipLaunchKernelGGL(fused_kernel, dim3(256), dim3(512), LDS_BYTES, stream, p);
}
```

```cpp
#include <hip/hip_runtime.h>
#include <hip/hip_bf16.h>
#include <stdint.h>
#include <type_traits>

namespace {

typedef unsigned short u16;
typedef unsigned v4u __attribute__((ext_vector_type(4)));
typedef unsigned v2u __attribute__((ext_vector_type(2)));
typedef float f32x4 __attribute__((ext_vector_type(4)));
typedef float f32x2 __attribute__((ext_vector_type(2)));
typedef __bf16 bf16x8 __attribute__((ext_vector_type(8)));
typedef __bf16 bf16x2 __attribute__((ext_vector_type(2)));
typedef short s16x4 __attribute__((ext_vector_type(4)));
#define LAS __attribute__((address_space(3)))
typedef LAS char* ldsp;

constexpr int D = 640, F = 1536;
constexpr unsigned WS_MB = 0;
constexpr unsigned WS_PQ = 65536;
constexpr unsigned WS_P2 = WS_PQ + 16u * 163840u;
constexpr unsigned WS_HA = WS_P2 + 8u * 348160u;
constexpr unsigned WS_END = WS_HA + 128u * 68u * 1024u;
constexpr int L_SLOTA = 28672;
constexpr int L_SLOTB = 32768;
constexpr int L_Q = 98304;
constexpr int L_HALO = 114688;
constexpr int L_SCR = 135168;
constexpr int L_KV = L_SCR;
constexpr int L_MISC = 159744;
constexpr int L_GW = L_MISC, L_B1 = L_MISC + 1280, L_B2 = L_MISC + 1472;
constexpr int L_G = L_MISC + 2048;
constexpr int L_CTL = L_MISC + 3072;
constexpr int L_RING2 = 10240;
constexpr int L_XH = 81920;
constexpr int L_XO = 122880;
constexpr int LDS_BYTES = 163840;
static_assert(L_Q + 128 * 88 <= L_HALO && L_KV + 144 * 168 <= L_MISC && L_XO + 20480 <= L_MISC, "lds");
constexpr int CTRL = 7;
constexpr unsigned SPIN_CAP = 1u << 18;

template <int I, int N, class Fn>
__device__ __forceinline__ void sfor(Fn&& f) {
  if constexpr (I < N) {
    f(std::integral_constant<int, I>{});
    sfor<I + 1, N>(f);
  }
}

template <int OFF, bool SC1>
__device__ __forceinline__ void dma_s(unsigned voff, const void* sbase, unsigned ldsaddr) {
  if constexpr (SC1)
    asm volatile("s_mov_b32 m0, %2\n\ts_nop 0\n\tglobal_load_lds_dwordx4 %0, %1 offset:%3 sc1" ::"v"(voff), "s"(sbase), "s"(ldsaddr), "n"(OFF) : "memory");
  else
    asm volatile("s_mov_b32 m0, %2\n\ts_nop 0\n\tglobal_load_lds_dwordx4 %0, %1 offset:%3" ::"v"(voff), "s"(sbase), "s"(ldsaddr), "n"(OFF) : "memory");
}
__device__ __forceinline__ void dma_s5(unsigned voff, const void* sbase_p2048, unsigned ldsaddr_p2048) {
  asm volatile(
      "s_mov_b32 m0, %2\n\ts_nop 0\n\t"
      "global_load_lds_dwordx4 %0, %1 offset:-2048 sc1\n\t"
      "global_load_lds_dwordx4 %0, %1 offset:-1024 sc1\n\t"
      "global_load_lds_dwordx4 %0, %1 sc1\n\t"
      "global_load_lds_dwordx4 %0, %1 offset:1024 sc1\n\t"
      "global_load_lds_dwordx4 %0, %1 offset:2048 sc1" ::"v"(voff), "s"(sbase_p2048), "s"(ldsaddr_p2048) : "memory");
}
__device__ __forceinline__ void dma_v(const void* vptr, unsigned ldsaddr) {
  asm volatile("s_mov_b32 m0, %1\n\ts_nop 0\n\tglobal_load_lds_dwordx4 %0, off" ::"v"(vptr), "s"(ldsaddr) : "memory");
}
template <int OFF>
__device__ __forceinline__ void st16_sc1(unsigned voff, const void* sbase, v4u d) {
  asm volatile("global_store_dwordx4 %0, %1, %2 offset:%3 sc1\n\ts_nop 1" ::"v"(voff), "v"(d), "s"(sbase), "n"(OFF) : "memory");
}
__device__ __forceinline__ void st4_sc1(unsigned voff, const void* sbase, unsigned d) {
  asm volatile("global_store_dword %0, %1, %2 sc1" ::"v"(voff), "v"(d), "s"(sbase) : "memory");
}
template <int OFF>
__device__ __forceinline__ void ld16_sc1(v4u& d, unsigned voff, const void* sbase) {
  asm volatile("global_load_dwordx4 %0, %1, %2 offset:%3 sc1" : "=v"(d) : "v"(voff), "s"(sbase), "n"(OFF) : "memory");
}
__device__ __forceinline__ unsigned ld4_sc1_wait(unsigned voff, const void* sbase) {
  unsigned d;
  asm volatile("global_load_dword %0, %1, %2 sc1\n\ts_waitcnt vmcnt(0)" : "=&v"(d) : "v"(voff), "s"(sbase) : "memory");
  return d;
}
__device__ __forceinline__ void ld4_sc1(unsigned& d, unsigned voff, const void* sbase) {
  asm volatile("global_load_dword %0, %1, %2 sc1" : "=v"(d) : "v"(voff), "s"(sbase) : "memory");
}
template <int N>
__device__ __forceinline__ void wait_vm_pin(unsigned& d) { asm volatile("s_waitcnt vmcnt(%1)" : "+v"(d) : "n"(N) : "memory"); }
template <int N>
__device__ __forceinline__ void wait_bar() {
  static_assert(N >= 0 && N <= 63, "vmcnt");
  asm volatile("s_waitcnt vmcnt(%0) lgkmcnt(0)\n\ts_barrier" ::"n"(N) : "memory");
}
__device__ __forceinline__ void lds_bar() { asm volatile("s_waitcnt lgkmcnt(0)\n\ts_barrier" ::: "memory"); }
template <int N>
__device__ __forceinline__ void wait_vm() {
  static_assert(N >= 0 && N <= 63, "vmcnt");
  asm volatile("s_waitcnt vmcnt(%0)" ::"n"(N) : "memory");
}
template <class T>
__device__ __forceinline__ void pin4(T& a, T& b, T& c, T& d) { asm volatile("" : "+v"(a), "+v"(b), "+v"(c), "+v"(d)); }
template <class T>
__device__ __forceinline__ void pin5(T& a, T& b, T& c, T& d, T& e) { asm volatile("" : "+v"(a), "+v"(b), "+v"(c), "+v"(d), "+v"(e)); }
template <class T>
__device__ __forceinline__ void lgkm0_pin5(T* x) {
  asm volatile("s_waitcnt lgkmcnt(0)" : "+v"(x[0]), "+v"(x[1]), "+v"(x[2]), "+v"(x[3]), "+v"(x[4])::"memory");
}

__device__ __forceinline__ v4u lds16(ldsp p, int off) { return *(const LAS v4u*)(p + off); }
__device__ __forceinline__ v2u lds8(ldsp p, int off) { return *(const LAS v2u*)(p + off); }
__device__ __forceinline__ v2u ldstr(ldsp p, int off) {
  return __builtin_bit_cast(v2u, __builtin_amdgcn_ds_read_tr16_b64_v4i16((LAS s16x4*)(p + off)));
}
__device__ __forceinline__ f32x4 mfma(v4u a, v4u b, f32x4 c) {
  return __builtin_amdgcn_mfma_f32_16x16x32_bf16(__builtin_bit_cast(bf16x8, a), __builtin_bit_cast(bf16x8, b), c, 0, 0, 0);
}
__device__ __forceinline__ unsigned pk2(float a, float b) {
  f32x2 v = {a, b};
  return __builtin_bit_cast(unsigned, __builtin_convertvector(v, bf16x2));
}
__device__ __forceinline__ float bflo(unsigned u) { return __uint_as_float(u << 16); }
__device__ __forceinline__ float bfhi(unsigned u) { return __uint_as_float(u & 0xffff0000u); }
__device__ __forceinline__ float rbf(float a) { return bflo(pk2(a, a)); }
__device__ __forceinline__ float dot2(unsigned a, unsigned b, float c) {
  return __builtin_amdgcn_fdot2_f32_bf16(__builtin_bit_cast(bf16x2, a), __builtin_bit_cast(bf16x2, b), c, false);
}
__device__ __forceinline__ float xor1(float v) {
  return __int_as_float(__builtin_amdgcn_update_dpp(0, __float_as_int(v), 0xB1, 0xF, 0xF, true));
}
__device__ __forceinline__ unsigned gelu2(f32x2 x) {
  const f32x2 c0 = {-2.3022081983f, -2.3022081983f}, c1 = {-0.1029432397f, -0.1029432397f};
  f32x2 z = x * (x * x * c1 + c0);
  f32x2 e = {__builtin_amdgcn_exp2f(z[0]), __builtin_amdgcn_exp2f(z[1])};
  e = e + 1.0f;
  f32x2 r = {__builtin_amdgcn_rcpf(e[0]), __builtin_amdgcn_rcpf(e[1])};
  f32x2 y = x * r;
  return pk2(y[0], y[1]);
}
#define SB() __builtin_amdgcn_sched_barrier(0)
__device__ __forceinline__ int fresh_lane() {
  unsigned z = 0;
  asm volatile("" : "+v"(z));
  return (int)__builtin_amdgcn_mbcnt_hi(~0u, __builtin_amdgcn_mbcnt_lo(~0u, z));
}
__device__ __forceinline__ int opaque(int v) {
  asm volatile("" : "+v"(v));
  return v;
}
__device__ __forceinline__ float dot2z(unsigned a, unsigned b) { return dot2(a, b, 0.f); }
__device__ __forceinline__ float addx1(float v) { return v + xor1(v); }
template <int OFF>
__device__ __forceinline__ void ldsr8(v2u& d, unsigned a) { asm volatile("ds_read_b64 %0, %1 offset:%2" : "=v"(d) : "v"(a), "n"(OFF) : "memory"); }

constexpr int JP = 2;
constexpr int JH = 2;
constexpr int JO = 1;
constexpr int NBV = 5;
constexpr int BV0 = 8;
constexpr int NAW = 3;
constexpr int KPOLL = 17;
constexpr int bv_lo(int kk) { return kk < BV0 ? 0 : (2 * (kk - BV0) < 5 * NBV ? 2 * (kk - BV0) : 5 * NBV); }
constexpr int bv_hi(int kk) { return kk == 20 ? 5 * NBV : bv_lo(kk + 1); }
struct Sched {
  int wa[10], wx, wb[5], wpoll, w2[9];
};
constexpr int imax(int a, int b) { return a > b ? a : b; }
constexpr Sched make_sched(int hf) {
  Sched s{};
  int n = 0, lastA[10] = {}, lastB[5] = {}, lastHalo = 0, lastH = 0, lastPk = 0, lastPs = 0;
  int B2[12] = {}, A2[12] = {};
  const int nA = hf == 0 ? 4 : 3, NK = hf == 0 ? 9 : 8;
  n += nA; lastA[0] = n;
  n += 1 + 3; lastPk = n;
  n += nA; lastA[1] = n;
  for (int i = 0; i < 10; ++i) {
    int need = i == 0 ? lastPk : lastA[i];
    if (i == JP) need = imax(need, lastPs);
    s.wa[i] = n - need;
    if (i == 0) { n += 3; lastPs = n; }
    if (i <= 7) { n += nA; lastA[i + 2] = n; }
    if (hf == 1 && i >= 3 && i <= 7) { n += 1; lastHalo = n; }
    if (i >= 8) { n += 2; lastB[0] = n; }
  }
  s.wx = n - imax(lastB[0], lastHalo);
  n += 4; lastB[1] = n;
  n += hf == 0 ? 4 : 2;
  lastH = n;
  int npoll = 0;
  for (int kk = 0; kk <= 20; ++kk) {
    const int j = kk >> 2;
    if ((kk & 3) == 0 && kk > 0 && kk < 20) {
      int need = lastB[j];
      if (j == JH) need = imax(need, lastH);
      s.wb[j] = n - need;
    }
    if (kk == KPOLL) npoll = n;
    if (j <= 2 && (kk & 3) >= 2) { n += 2; lastB[j + 2] = n; }
    for (int f = bv_lo(kk); f < bv_hi(kk); ++f) { n += 1; B2[f / 5] = n; }
  }
  s.wpoll = n - npoll;
  for (int i = 0; i < 2; ++i) { n += 4; A2[i] = n; }
  n += 2;
  const int lastO = n;
  for (int i = 2; i < NAW; ++i) { n += 4; A2[i] = n; }
  for (int i = NBV; i < NBV + 2; ++i) { n += 5; B2[i] = n; }
  for (int i = 0; i < NK; ++i) {
    if (hf == 0 && i == 4) for (int k = 4; k < 9; ++k) { n += 4; A2[k] = n; }
    int need = imax(A2[i], B2[i]);
    if (i + 1 < NK && i + 1 >= NBV) need = imax(need, B2[i + 1]);
    if (i == JO) need = imax(need, lastO);
    s.w2[i] = n - need;
    if (i >= NBV && i + 2 < NK) { n += 5; B2[i + 2] = n; }
    if (i + NAW < (hf == 0 ? 4 : NK)) { n += 4; A2[i + NAW] = n; }
  }
  return s;
}

struct Params {
  const u16* x;
  const u16* wqkv;
  const u16* wo;
  const u16* w1;
  const u16* b1;
  const u16* w2;
  const u16* b2;
  const u16* gw;
  u16* out;
  char* ws;
};

#define STAMP(i)
#define FST(i)

__device__ __forceinline__ void poll_mail(const char* ws, unsigned mb_off, unsigned lvl, unsigned ack_off, bool ack_on,
                                          unsigned ack_val) {
  for (unsigned spin = 0; spin < SPIN_CAP; ++spin) {
    unsigned v = ld4_sc1_wait(mb_off, ws);
    bool ok = v >= lvl && v <= 2u;
    if (__all(ok)) break;
    if (ack_on) st4_sc1(ack_off, ws, ack_val);
    __builtin_amdgcn_s_sleep(2);
  }
}

template <int N>
using IC = std::integral_constant<int, N>;

template <int HF>
__device__ __forceinline__ void body(const Params& p, ldsp lds, const int w, int lane, const int rb, const int cg) {
  constexpr Sched SC = make_sched(HF);
  constexpr bool OW = HF == 0;
  const int s = w & 3;
  int r16 = lane & 15, g = lane >> 4;
  const int ct = cg & 7, hf2 = cg >> 3;
  const char* ws = p.ws;
  const unsigned ldsb = (unsigned)(size_t)lds;
  unsigned l16 = lane * 16;
  auto relane = [&]() { lane = fresh_lane(); r16 = lane & 15; g = lane >> 4; l16 = lane * 16; };
  const int rot = (rb + 3 * cg) % 10;
  auto se = [&](int st) { const int t = st + rot; return t >= 10 ? t - 10 : t; };
  const unsigned xo0 = (lane >> 3) * 1280 + (((lane & 7) ^ (lane >> 4)) * 16);
  const unsigned xo1 = (8 + (lane >> 3)) * 1280 + (((lane & 7) ^ (4 + (lane >> 4))) * 16);
  const char* xb = (const char*)p.x + (size_t)(rb * 128 + 16 * w) * 1280;
  unsigned wo_[2];
  sfor<0, 2 - HF>([&](auto I) {
    const int P = HF == 0 ? s + 4 * I : 8 + s;
    const int Q = 64 * P + lane, rowp = Q / 12, pos = Q % 12;
    const int c = (pos >> 1) ^ ((rowp >> 2) & 1);
    const int r = (rowp & 0x33) | ((rowp & 4) << 1) | ((rowp & 8) >> 1);
    wo_[I] = r * 3072 + c * 32 + (pos & 1) * 16;
  });
  const char* w1b = (const char*)p.w1 + cg * 192;
  auto issueA = [&](int st, int part) {
    const unsigned sl = ldsb + (st & 3) * L_SLOTA;
    const int e = se(st);
    if (part == 0) dma_s<0, false>(xo0, xb + e * 128, sl + w * 2048);
    if (part == 1) dma_s<0, false>(xo1, xb + e * 128, sl + w * 2048 + 1024);
    if (part == 2) dma_s<0, false>(wo_[0], w1b + e * 196608, sl + 16384 + (HF == 0 ? s : 8 + s) * 1024);
    if constexpr (HF == 0)
      if (part == 3) dma_s<0, false>(wo_[1], w1b + e * 196608, sl + 16384 + (s + 4) * 1024);
  };
  issueA(0, 0); issueA(0, 1); issueA(0, 2); issueA(0, 3);
  const unsigned myL = rb * 16 + cg;
  auto mbp = [&](int l) -> unsigned { return WS_MB + myL * 256 + (l & 31) * 4; };
  auto ackp = [&](int l) -> unsigned { return WS_MB + (((l & 31) >> 1) * 16 + ct + 8 * (l & 1)) * 256 + (rb * 2 + hf2) * 4; };
  auto mbr = [&](int l) -> unsigned { return WS_MB + myL * 256 + (32 + (l & 15)) * 4; };
  auto ackr = [&](int l) -> unsigned { return WS_MB + (rb * 16 + (l & 15)) * 256 + (32 + cg) * 4; };
  if (HF == 1 && w == CTRL) {
    if (lane < 48) st4_sc1(WS_MB + myL * 256 + lane * 4, ws, 0u);
    if (lane < 8) *(LAS unsigned*)(lds + L_CTL + lane * 4) = 0u;
  }
  {
    const int c = 16 * w + r16;
    const u16* src = c < 80 ? p.gw + c * 8 : c < 92 ? p.b1 + cg * 96 + (c - 80) * 8 : c < 102 ? p.b2 + ct * 80 + (c - 92) * 8 : p.gw;
    if (lane < 16) dma_v(src, ldsb + L_MISC + w * 256);
  }
  const int jme = rb * 2 + hf2;
  const int gbase = jme < 16 ? 9 * jme : 144 + 8 * (jme - 16), gcnt = jme < 16 ? 9 : 8;
  sfor<0, 3>([&](auto I) {
    const int Q = (3 * w + I) * 64 + lane;
    const u16* src;
    if (Q < 600) {
      const int gi = Q / 120, rem = Q % 120, row = rem / 15, ch = rem % 15, seg = ch / 5, c5 = ch % 5;
      src = p.wqkv + ((5 * rb + gi) * 8 + row) * 1920 + (seg == 0 ? 640 : seg == 1 ? 1280 : 0) + cg * 40 + c5 * 8;
    } else {
      const int Qp = Q < 1320 ? Q - 600 : 0;
      int gi = Qp / 80;
      const int rem = Qp % 80, row = rem / 10, ch = rem % 10;
      gi = gi < gcnt ? gi : gcnt - 1;
      const int G = gbase + gi, t = G >> 2, gg = G & 3;
      src = (t < 48 ? p.w2 + (32 * t + 16 * (row >> 2) + 4 * gg + (row & 3)) * 640 : p.wo + (32 * (t - 48) + 8 * gg + row) * 640) + ct * 80 + ch * 8;
    }
    dma_v(src, ldsb + L_SCR + (3 * w + I) * 1024);
  });
  issueA(1, 0); issueA(1, 1); issueA(1, 2); issueA(1, 3);
  const char* pqb = ws + WS_PQ + cg * 163840 + w * 2048;
  auto issueB = [&](int jb, int q) {
    dma_s<0, true>(l16, pqb + se(2 * jb + (q >> 1)) * 16384 + (q & 1) * 1024, ldsb + ((jb + 2) % 3) * L_SLOTB + (q >> 1) * 16384 + w * 2048 + (q & 1) * 1024);
  };
  STAMP(0);

  const int xa = (32 * s + r16) * 128 + ((g ^ (r16 >> 1)) * 16);
  const int q4 = r16 >> 2, p4 = lane & 3;
  const int wa = 16384 + 192 * (16 * (g >> 1) + 4 * (g & 1) + q4) + 8 * p4;
  constexpr int HI = 2 * L_SLOTA;
  const int xab[2][2] = {{opaque(xa), opaque(xa ^ 64)}, {opaque(xa + HI), opaque((xa ^ 64) + HI)}};
  const int wab[2][2] = {{opaque(wa + 32 * (g & 1)), opaque(wa - 32 * (g & 1))}, {opaque(wa + 32 * (g & 1) + HI), opaque(wa - 32 * (g & 1) + HI)}};

  v4u xf[2][20];
  constexpr int NA = HF == 0 ? 4 : 2;
  f32x4 acc[2][NA];
  unsigned pollv = 0;
  {
    v4u bf[2][NA];
    sfor<0, 21>([&](auto KK) {
      constexpr int kk = KK, i = kk >> 1, ks = kk & 1;
      constexpr int sl = (i & 1) * L_SLOTA, hb = (i >> 1) & 1;
      auto rdx = [&](auto M) {
        if constexpr (kk < 20) xf[M][kk] = lds16(lds, sl + M * 2048 + xab[hb][ks]);
      };
      auto rdb = [&](auto N) {
        if constexpr (kk < 20) {
          constexpr int c = HF == 0 ? (int)N : 4 + N;
          const int a0 = sl + wab[hb][c & 1] + 192 * 32 * ks + 32 * c;
          const v2u lo = ldstr(lds, a0), hi = ldstr(lds, a0 + 192 * 8);
          bf[ks][N] = v4u{lo[0], lo[1], hi[0], hi[1]};
        }
      };
      auto mm = [&](auto N, auto M) {
        if constexpr (kk >= 1) acc[M][N] = mfma(bf[(kk - 1) & 1][N], xf[M][kk - 1], acc[M][N]);
      };
      auto dma = [&](auto Q) {
        constexpr int part = 2 * ks + Q;
        if constexpr (kk < 20) {
          if constexpr (i <= 7) issueA(i + 2, part);
          if constexpr (HF == 1 && part == 3 && i >= 3 && i <= 7) {
            const int l = fresh_lane();
            const int hrow = rb * 128 - 16 + 8 * (s & 1) + (l >> 3);
            const unsigned ho = (hrow > 0 ? hrow : 0) * 1280 + (((l & 7) ^ (4 * (s & 1) + (l >> 4))) * 16);
            const int st = 2 * (i - 3) + (s >> 1);
            dma_s<0, false>(ho, (const char*)p.x + se(st) * 128, ldsb + L_HALO + st * 2048 + (s & 1) * 1024);
          }
          if constexpr (i >= 8 && Q == 0) issueB(0, 2 * (i - 8) + ks);
        }
      };
      if constexpr (kk < 20 && ks == 0) {
        FST(2 * i);
        wait_bar<SC.wa[i]>();
        FST(2 * i + 1);
        if constexpr (i == 0) {
          STAMP(1);
          sfor<0, 3>([&](auto J) {
            int U = (3 * w + J) * 4 + g;
            U = U < 85 ? U : U - 11;
            int la;
            unsigned dst;
            const int Gq = 5 * rb + (U < 40 ? U >> 3 : 0);
            if (U < 40) {
              const int gi = U >> 3, nt = U & 7;
              la = L_SCR + 1920 * gi + q4 * 240 + nt * 32 + p4 * 8;
              dst = WS_PQ + cg * 163840 + (Gq >> 2) * 8192 + nt * 1024 + ((Gq & 3) * 16 + r16) * 16;
            } else {
              const int Up = U - 40, gi = Up / 5, nt = Up % 5, G = gbase + (gi < gcnt ? gi : gcnt - 1);
              la = L_SCR + 9600 + 1280 * gi + q4 * 160 + nt * 32 + p4 * 8;
              dst = WS_P2 + ct * 348160 + (G >> 2) * 5120 + nt * 1024 + ((G & 3) * 16 + r16) * 16;
            }
            const int pitch4 = U < 40 ? 960 : 640;
            const v2u lo = ldstr(lds, la), hi = ldstr(lds, la + pitch4);
            v4u d = v4u{lo[0], lo[1], hi[0], hi[1]};
            if constexpr (HF == 0) {
              const v4u gv = lds16(lds, L_GW + Gq * 16);
              if (U < 40 && (U & 7) == 7 && r16 == 8) d = gv;
            }
            st16_sc1<0>(dst, ws, d);
          });
          sfor<0, NA>([&](auto N) {
            const v2u bb = lds8(lds, L_B1 + ((HF == 0 ? 0 : 4) + N) * 32 + g * 8);
            acc[0][N] = acc[1][N] = f32x4{bflo(bb[0]), bfhi(bb[0]), bflo(bb[1]), bfhi(bb[1])};
          });
        }
        if constexpr (HF == 1) {
          if constexpr (i == JP) {
            if (w == CTRL) {
              const int l = fresh_lane();
              if (l < 32) st4_sc1(ackp(l), ws, 1u);
            }
          }
          if constexpr (i == 5) ld4_sc1(pollv, mbp(fresh_lane()), ws);
          if constexpr (i == 7) {
            wait_vm_pin<SC.wa[7]>(pollv);
            if (w == CTRL) {
              STAMP(2);
              const int l = fresh_lane();
              if (!__all(pollv - 1u < 2u)) poll_mail(ws, mbp(l), 1u, ackp(l), l < 32, 1u);
              if (l < 32) st4_sc1(ackp(l), ws, 1u);
              STAMP(3);
            }
          }
        }
        SB();
      }
      if constexpr (HF == 0) {
        rdx(IC<0>{}); mm(IC<0>{}, IC<0>{}); SB();
        rdx(IC<1>{}); mm(IC<0>{}, IC<1>{}); SB();
        rdb(IC<0>{}); mm(IC<1>{}, IC<0>{}); SB();
        dma(IC<0>{}); mm(IC<1>{}, IC<1>{}); SB();
        rdb(IC<1>{}); mm(IC<2>{}, IC<0>{}); SB();
        rdb(IC<2>{}); mm(IC<2>{}, IC<1>{}); SB();
        rdb(IC<3>{}); mm(IC<3>{}, IC<0>{}); SB();
        dma(IC<1>{}); mm(IC<3>{}, IC<1>{}); SB();
      } else {
        rdx(IC<0>{}); rdx(IC<1>{}); mm(IC<0>{}, IC<0>{}); SB();
        rdb(IC<0>{}); mm(IC<0>{}, IC<1>{}); SB();
        dma(IC<0>{}); rdb(IC<1>{}); mm(IC<1>{}, IC<0>{}); SB();
        dma(IC<1>{}); mm(IC<1>{}, IC<1>{}); SB();
      }
    });
  }
  wait_bar<SC.wx>();
  STAMP(4);
  relane();
  issueB(1, 0); issueB(1, 1); issueB(1, 2); issueB(1, 3);
  {
    const char* hab = ws + WS_HA + (size_t)((8 * rb + 2 * s) * 68 + 3 * cg + 2 * HF) * 1024;
    sfor<0, 2>([&](auto M) {
      sfor<0, NA / 2>([&](auto T) {
        v4u o;
        sfor<0, 2>([&](auto Hh) {
          const f32x4 a = acc[M][2 * T + Hh];
          o[2 * Hh] = gelu2(f32x2{a[0], a[1]});
          o[2 * Hh + 1] = gelu2(f32x2{a[2], a[3]});
        });
        st16_sc1<T * 1024>(l16, hab + M * 69632, o);
      });
    });
  }
  STAMP(5);
  constexpr int NK = OW ? 9 : 8;
  const int k0 = OW ? 4 * w : 16 + 8 * (w - 4), k1o = 48 + 5 * w;
  const int r2 = rb + 2 * (cg & 3);
  auto kk2 = [&](int i) {
    if (!OW) return k0 + ((i + r2) & 7);
    if (i < 4) return k0 + ((i + r2) & 3);
    const int t = i - 4 + r2 % 5;
    return k1o + (t >= 5 ? t - 5 : t);
  };
  const char* bb2 = ws + WS_P2 + ct * 348160 + 2048;
  v4u bv[NBV][5];
  auto issueBv = [&](auto Fq) {
    constexpr int f = Fq;
    ld16_sc1<(f % 5) * 1024 - 2048>(bv[f / 5][f % 5], l16, bb2 + kk2(f / 5) * 5120);
  };
  constexpr int NB = HF == 0 ? 3 : 5;
  f32x4 ac2[2][NB];
  f32x4 ach[2];
  sfor<0, 2>([&](auto M) {
    sfor<0, NB>([&](auto N) { ac2[M][N] = f32x4{0, 0, 0, 0}; });
    ach[M] = f32x4{0, 0, 0, 0};
  });
  {
    const int ha0 = L_HALO + r16 * 128 + ((g ^ (r16 >> 1)) * 16);
    const int hab[2] = {opaque(ha0), opaque(ha0 ^ 64)};
    const int lb[2] = {(int)l16, opaque(l16 + 2 * L_SLOTB)};
    const int lbs[2] = {opaque(l16 + s * 1024), opaque(l16 + s * 1024 + 2 * L_SLOTB)};
    v4u bf[2][NB];
    v4u hq[3];
    sfor<0, 21>([&](auto KK) {
      constexpr int kk = KK, j = kk >> 2, ks = kk & 1;
      constexpr int slot = (j + 2) % 3, hb = slot >> 1, sl = (slot & 1) * L_SLOTB + (kk & 3) * 8192;
      auto rdb = [&](auto N) {
        if constexpr (kk < 20) bf[ks][N] = lds16(lds, sl + ((HF == 0 ? 5 : 0) + N) * 1024 + lb[hb]);
      };
      auto rdh = [&](auto N) {
        if constexpr (kk < 20) {
          if constexpr (N == 0) hq[0] = lds16(lds, (kk >> 1) * 2048 + hab[ks]);
          if constexpr (N == 1) hq[1] = lds16(lds, sl + lbs[hb]);
          if constexpr (N == 2) hq[2] = lds16(lds, sl + 4096 + lb[hb]);
        }
      };
      auto mm = [&](auto N, auto M) {
        if constexpr (kk >= 1) ac2[M][N] = mfma(bf[(kk - 1) & 1][N], xf[M][kk - 1], ac2[M][N]);
      };
      auto mh = [&](auto M) {
        if constexpr (kk < 20) ach[M] = mfma(hq[1 + M], hq[0], ach[M]);
      };
      auto dma = [&](auto Q) {
        if constexpr (j <= 2 && (kk & 3) >= 2) issueB(j + 2, 2 * (kk & 1) + Q);
      };
      auto pre = [&](auto Q) {
        if constexpr (bv_lo(kk) + Q < bv_hi(kk)) issueBv(IC<bv_lo(kk) + Q>{});
      };
      if constexpr ((kk & 3) == 0 && kk > 0 && kk < 20) {
        FST(20 + 2 * j);
        wait_bar<SC.wb[j]>();
        FST(21 + 2 * j);
        if constexpr (j == JH && HF == 1) {
          if (w == CTRL) {
            const int l = fresh_lane();
            if (l < 16) st4_sc1(ackr(l), ws, 1u);
          }
        }
        SB();
      }
      if constexpr (kk == KPOLL && HF == 1) { ld4_sc1(pollv, mbr(fresh_lane()), ws); SB(); }
      if constexpr (HF == 0) {
        rdh(IC<0>{}); rdh(IC<1>{}); mm(IC<0>{}, IC<0>{}); SB();
        rdh(IC<2>{}); mm(IC<0>{}, IC<1>{}); SB();
        rdb(IC<0>{}); mm(IC<1>{}, IC<0>{}); SB();
        dma(IC<0>{}); mm(IC<1>{}, IC<1>{}); SB();
        rdb(IC<1>{}); mm(IC<2>{}, IC<0>{}); SB();
        dma(IC<1>{}); rdb(IC<2>{}); mm(IC<2>{}, IC<1>{}); SB();
        pre(IC<0>{}); mh(IC<0>{}); SB();
        pre(IC<1>{}); mh(IC<1>{}); SB();
      } else {
        rdb(IC<0>{}); mm(IC<0>{}, IC<0>{}); SB();
        mm(IC<0>{}, IC<1>{}); SB();
        rdb(IC<1>{}); mm(IC<1>{}, IC<0>{}); SB();
        dma(IC<0>{}); mm(IC<1>{}, IC<1>{}); SB();
        rdb(IC<2>{}); mm(IC<2>{}, IC<0>{}); SB();
        dma(IC<1>{}); mm(IC<2>{}, IC<1>{}); SB();
        rdb(IC<3>{}); mm(IC<3>{}, IC<0>{}); SB();
        mm(IC<3>{}, IC<1>{}); SB();
        rdb(IC<4>{}); mm(IC<4>{}, IC<0>{}); SB();
        mm(IC<4>{}, IC<1>{}); SB();
        pre(IC<0>{}); pre(IC<1>{}); SB();
      }
    });
    static_assert(bv_hi(20) - bv_lo(20) <= 2, "bv tail");
  }
  STAMP(6);
  relane();
  {
    auto put = [&](int off, f32x4 a) { *(LAS v2u*)(lds + off) = v2u{pk2(a[0], a[1]), pk2(a[2], a[3])}; };
    if constexpr (HF == 1) {
      sfor<0, 2>([&](auto M) {
        sfor<0, 5>([&](auto N) { put(L_KV + (16 + 32 * s + 16 * M + r16) * 168 + 32 * N + 8 * g, ac2[M][N]); });
      });
    } else {
      sfor<0, 2>([&](auto M) {
        sfor<0, 2>([&](auto N) { put(L_Q + (32 * s + 16 * M + r16) * 88 + 32 * N + 8 * g, ac2[M][N]); });
        if (g < 2) put(L_Q + (32 * s + 16 * M + r16) * 88 + 64 + 8 * g, ac2[M][2]);
        const float t = rbf(ac2[M][2][0]);
        const float gg = rbf(__builtin_amdgcn_rcpf(1.0f + __builtin_amdgcn_exp2f(-1.4426950409f * t)));
        if (g == 2) *(LAS f32x2*)(lds + L_G + (32 * s + 16 * M + r16) * 8) = f32x2{gg, rbf(1.0f - gg)};
      });
      put(L_KV + r16 * 168 + 32 * s + 8 * g, ach[0]);
      if (s == 0) put(L_KV + r16 * 168 + 128 + 8 * g, ach[1]);
    }
  }
  const unsigned mb2 = mbr(lane), ack2 = ackr(lane);
  if constexpr (HF == 1) {
    wait_vm_pin<SC.wpoll>(pollv);
    if (w == CTRL) {
      STAMP(7);
      if (!__all(pollv - 1u < 2u)) poll_mail(ws, mb2, 1u, ack2, lane < 16, 1u);
      if (lane < 16) st4_sc1(ack2, ws, 1u);
      STAMP(8);
    }
  }
  lds_bar();
  STAMP(9);
  const unsigned ring = ldsb + w * L_RING2 + 2048;
  auto issueB2 = [&](auto I) {
    constexpr int i = I;
    dma_s5(l16, bb2 + kk2(i) * 5120, ring + ((i - NBV) & 1) * 5120);
  };
  const char* ab = ws + WS_HA + (size_t)(8 * rb + 4 * hf2) * 69632;
  v4u af[NK][4];
  auto issueA2 = [&](auto I) {
    constexpr int i = I;
    const char* a = ab + kk2(i) * 1024;
    sfor<0, 4>([&](auto M) { ld16_sc1<0>(af[i][M], l16, a + M * 69632); });
  };
  {
    const int row = 16 * w + (lane >> 2), hh = (lane >> 1) & 1, wh = lane & 1;
    const unsigned qa = ldsb + L_Q + row * 88 + hh * 40;
    const unsigned ka = ldsb + L_KV + (10 + row - 8 * wh) * 168 + hh * 40;
    v2u q[5], k[4][5], v[4][5];
    sfor<0, 5>([&](auto C) { ldsr8<C * 8>(q[C], qa); });
    sfor<0, 4>([&](auto I) { sfor<0, 5>([&](auto C) { ldsr8<(3 - I) * 336 + C * 8>(k[I][C], ka); }); });
    const float gg = *(const LAS float*)(lds + L_G + row * 8);
    issueA2(IC<0>{});
    asm volatile("s_waitcnt lgkmcnt(0)" ::: "memory");
    sfor<0, 5>([&](auto C) { pin5(q[C], k[0][C], k[1][C], k[2][C], k[3][C]); });
    sfor<0, 4>([&](auto I) { sfor<0, 5>([&](auto C) { ldsr8<80 + (3 - I) * 336 + C * 8>(v[I][C], ka); }); });
    float lg[4];
    sfor<0, 4>([&](auto I) {
      float a = dot2z(q[0][0], k[I][0][0]);
      a = dot2(q[0][1], k[I][0][1], a);
      sfor<1, 5>([&](auto C) {
        a = dot2(q[C][0], k[I][C][0], a);
        a = dot2(q[C][1], k[I][C][1], a);
      });
      a *= 0.22360679774997896f * 1.4426950408889634f;
      lg[I] = (rb * 128 + row - 8 * wh - 2 * I >= 0) ? a : -1e9f * 1.4426950408889634f;
    });
    float mx = fmaxf(fmaxf(lg[0], lg[1]), fmaxf(lg[2], lg[3]));
    mx = fmaxf(mx, xor1(mx));
    float e[4], sum = 0.f;
    sfor<0, 4>([&](auto I) { e[I] = __builtin_amdgcn_exp2f(lg[I] - mx); sum += e[I]; });
    sum = addx1(sum);
    const float inv = __builtin_amdgcn_rcpf(sum);
    const unsigned p01 = pk2(e[0] * inv, e[1] * inv), p23 = pk2(e[2] * inv, e[3] * inv);
    issueA2(IC<1>{});
    asm volatile("s_waitcnt lgkmcnt(0)" ::: "memory");
    sfor<0, 5>([&](auto C) { pin4(v[0][C], v[1][C], v[2][C], v[3][C]); });
    unsigned ov[10];
    sfor<0, 5>([&](auto C) {
      sfor<0, 2>([&](auto E_) {
        constexpr int E = E_;
        float lo = dot2z(__builtin_amdgcn_perm(v[1][C][E], v[0][C][E], 0x05040100u), p01);
        float hi = dot2z(__builtin_amdgcn_perm(v[1][C][E], v[0][C][E], 0x07060302u), p01);
        lo = dot2(__builtin_amdgcn_perm(v[3][C][E], v[2][C][E], 0x05040100u), p23, lo);
        hi = dot2(__builtin_amdgcn_perm(v[3][C][E], v[2][C][E], 0x07060302u), p23, hi);
        ov[2 * C + E] = pk2(addx1(lo) * gg, addx1(hi) * gg);
      });
    });
    const int oa = L_Q + row * 88 + hh * 40 + wh * 24;
    *(LAS v2u*)(lds + oa) = wh ? v2u{ov[6], ov[7]} : v2u{ov[0], ov[1]};
    *(LAS v2u*)(lds + oa + 8) = wh ? v2u{ov[8], ov[9]} : v2u{ov[2], ov[3]};
    if (!wh) *(LAS v2u*)(lds + oa + 16) = v2u{ov[4], ov[5]};
    const char* ob = ws + WS_HA + (size_t)((8 * rb + w) * 68 + 48) * 1024 + cg * 1280;
    asm volatile("s_waitcnt lgkmcnt(0)" ::: "memory");
    sfor<0, 2>([&](auto I) {
      const int c = I * 40 + (lane < 40 ? lane : 0);
      const int la = L_Q + (16 * w + (c & 15)) * 88 + (c >> 4) * 16;
      const v2u a = lds8(lds, la), b = lds8(lds, la + 8);
      if (lane < 40) st16_sc1<0>(c * 16, ob, v4u{a[0], a[1], b[0], b[1]});
    });
  }
  STAMP(10);
  sfor<2, NAW>(issueA2);
  sfor<NBV, NBV + 2>(issueB2);
  f32x4 acc2[4][5];
  sfor<0, 4>([&](auto M) { sfor<0, 5>([&](auto N) { acc2[M][N] = f32x4{0, 0, 0, 0}; }); });
  const int rg = w * L_RING2 + l16;
  v4u bl[2][5];
  sfor<0, NK>([&](auto I) {
    constexpr int i = I;
    if constexpr (OW && i == 4) {
      STAMP(11);
      for (unsigned spin = 0; spin < SPIN_CAP * 16; ++spin) {
        const unsigned f = *(volatile LAS unsigned*)(lds + L_CTL + 16 + w * 4);
        if (__builtin_amdgcn_readfirstlane(f) != 0u) break;
        __builtin_amdgcn_s_sleep(1);
      }
      asm volatile("" ::: "memory");
      sfor<0, 4>([&](auto M) {
        const float og = *(const LAS float*)(lds + L_G + (64 * hf2 + 16 * M + r16) * 8 + 4);
        sfor<0, 5>([&](auto N) {
          constexpr int t = 5 * M + N;
          const int a = t < 10 ? (w + 4) * L_RING2 + t * 1024 + l16 : L_XH + w * 10240 + (t - 10) * 1024 + l16;
          acc2[M][N] = (acc2[M][N] + __builtin_bit_cast(f32x4, lds16(lds, a))) * og;
        });
      });
      STAMP(12);
      poll_mail(ws, mb2, 2u, ack2, false, 0u);
      STAMP(13);
      sfor<4, 9>(issueA2);
    }
    FST(40 + 2 * i);
    wait_vm<SC.w2[i]>();
    FST(41 + 2 * i);
    pin4(af[i][0], af[i][1], af[i][2], af[i][3]);
    if constexpr (i < NBV) pin5(bv[i][0], bv[i][1], bv[i][2], bv[i][3], bv[i][4]);
    if constexpr (i == JO) {
      unsigned old = 0;
      if (lane == 0) old = __hip_atomic_fetch_add((LAS unsigned*)(lds + L_CTL), 1u, __ATOMIC_RELAXED, __HIP_MEMORY_SCOPE_WORKGROUP);
      if (__builtin_amdgcn_readfirstlane(old) == 7u) {
        if (lane < 16) st4_sc1(ack2, ws, 2u);
      }
    }
    if constexpr (i >= NBV && i + 2 < NK) {
      lgkm0_pin5(bl[i & 1]);
      issueB2(IC<i + 2>{});
    }
    SB();
    sfor<0, 4>([&](auto M) {
      sfor<0, 5>([&](auto N) {
        if constexpr (M == 0 && i + 1 < NK && i + 1 >= NBV) bl[(i + 1) & 1][N] = lds16(lds, rg + ((i + 1 - NBV) & 1) * 5120 + N * 1024);
        if constexpr (i < NBV) acc2[M][N] = mfma(bv[i][N], af[i][M], acc2[M][N]);
        else acc2[M][N] = mfma(bl[i & 1][N], af[i][M], acc2[M][N]);
        if constexpr (M == 0) SB();
      });
    });
    SB();
    if constexpr (i + NAW < (OW ? 4 : NK)) issueA2(IC<i + NAW>{});
  });
  STAMP(14);
  if constexpr (!OW) {
    for (unsigned spin = 0; spin < SPIN_CAP * 16; ++spin) {
      const unsigned f = *(volatile LAS unsigned*)(lds + L_CTL);
      if (__builtin_amdgcn_readfirstlane(f) >= 8u) break;
      __builtin_amdgcn_s_sleep(1);
    }
    asm volatile("" ::: "memory");
    sfor<0, 20>([&](auto T) {
      constexpr int t = T;
      const int a = t < 10 ? w * L_RING2 + t * 1024 + l16 : L_XH + (w - 4) * 10240 + (t - 10) * 1024 + l16;
      *(LAS f32x4*)(lds + a) = acc2[t / 5][t % 5];
    });
    asm volatile("s_waitcnt lgkmcnt(0)" ::: "memory");
    if (lane == 0) *(volatile LAS unsigned*)(lds + L_CTL + 16 + (w - 4) * 4) = 1u;
    lds_bar();
  } else {
    auto xbuf = [&](int src, int slot) { return slot < 2 ? src * L_RING2 + slot * 5120 : L_XO + src * 5120; };
    sfor<0, 4>([&](auto M) {
      constexpr int m = M;
      if (m != w) {
        const int a = xbuf(w, m < w ? m : m - 1) + l16;
        sfor<0, 5>([&](auto N) { *(LAS f32x4*)(lds + a + N * 1024) = acc2[m][N]; });
      }
    });
    f32x4 own[5];
    sfor<0, 5>([&](auto N) { own[N] = w == 0 ? acc2[0][N] : w == 1 ? acc2[1][N] : w == 2 ? acc2[2][N] : acc2[3][N]; });
    lds_bar();
    sfor<1, 4>([&](auto Dd) {
      const int o = (w + Dd) & 3;
      const int a = xbuf(o, w < o ? w : w - 1) + l16;
      sfor<0, 5>([&](auto N) { own[N] += __builtin_bit_cast(f32x4, lds16(lds, a + N * 1024)); });
    });
    const int row = 64 * hf2 + 16 * w + r16;
    const float og = *(const LAS float*)(lds + L_G + row * 8 + 4);
    u16* op = p.out + (size_t)(rb * 128 + row) * 640 + ct * 80 + g * 4;
    sfor<0, 5>([&](auto N) {
      const v2u b = lds8(lds, L_B2 + N * 32 + g * 8);
      const f32x4 a = own[N];
      *(v2u*)(op + N * 16) = v2u{pk2(a[0] + og * bflo(b[0]), a[1] + og * bfhi(b[0])), pk2(a[2] + og * bflo(b[1]), a[3] + og * bfhi(b[1]))};
    });
  }
  STAMP(15);
}

__global__ __launch_bounds__(512, 2) void fused_kernel(Params p) {
  extern __shared__ __attribute__((aligned(16))) char smem[];
  ldsp lds = (ldsp)smem;
  const int tid = threadIdx.x, lane = tid & 63;
  const int w = __builtin_amdgcn_readfirstlane(tid >> 6);
  const int b = blockIdx.x, xcd = b & 7, idx = b >> 3;
  const int rb = (xcd >> 2) * 8 + (idx & 7), cg = (xcd & 3) * 4 + (idx >> 3);
  if (w < 4) {
    body<0>(p, lds, w, lane, rb, cg);
  } else {
    body<1>(p, lds, w, lane, rb, cg);
  }
}

}

extern "C" void kernel_launch(void* const* d_in, const int* in_sizes, int n_in, void* d_out, int out_size, void* d_ws, size_t ws_size,
                              hipStream_t stream) {
  Params p;
  p.x = (const u16*)d_in[0];
  p.wqkv = (const u16*)d_in[1];
  p.wo = (const u16*)d_in[2];
  p.w1 = (const u16*)d_in[4];
  p.b1 = (const u16*)d_in[5];
  p.w2 = (const u16*)d_in[6];
  p.b2 = (const u16*)d_in[7];
  p.gw = (const u16*)d_in[8];
  p.out = (u16*)d_out;
  p.ws = (char*)d_ws;
  (void)hipFuncSetAttribute((const void*)fused_kernel, hipFuncAttributeMaxDynamicSharedMemorySize, LDS_BYTES);
  hipLaunchKernelGGL(fused_kernel, dim3(256), dim3(512), LDS_BYTES, stream, p);
}
```

```cpp
#include <hip/hip_runtime.h>
#include <hip/hip_bf16.h>
#include <stdint.h>
#include <type_traits>

namespace {

typedef unsigned short u16;
typedef unsigned v4u __attribute__((ext_vector_type(4)));
typedef unsigned v2u __attribute__((ext_vector_type(2)));
typedef float f32x4 __attribute__((ext_vector_type(4)));
typedef float f32x2 __attribute__((ext_vector_type(2)));
typedef __bf16 bf16x8 __attribute__((ext_vector_type(8)));
typedef __bf16 bf16x2 __attribute__((ext_vector_type(2)));
typedef short s16x4 __attribute__((ext_vector_type(4)));
#define LAS __attribute__((address_space(3)))
typedef LAS char* ldsp;

constexpr unsigned WS_MB = 0;
constexpr unsigned WS_PQ = 65536;
constexpr unsigned WS_P2 = WS_PQ + 16u * 163840u;
constexpr unsigned WS_HA = WS_P2 + 8u * 348160u;
constexpr unsigned WS_END = WS_HA + 128u * 68u * 1024u;
static_assert(WS_END < (64u << 20), "workspace");
constexpr int L_SLOTA = 28672;
constexpr int L_SLOTB = 32768;
constexpr int L_Q = 98304;
constexpr int L_HALO = 114688;
constexpr int L_SCR = 135168;
constexpr int L_KV = L_SCR;
constexpr int L_MISC = 159744;
constexpr int L_GW = L_MISC, L_B1 = L_MISC + 1280, L_B2 = L_MISC + 1472;
constexpr int L_G = L_MISC + 2048;
constexpr int L_CTL = L_MISC + 3072;
constexpr int L_RO = 15360;
constexpr int L_RH = 61440;
constexpr int L_XO = L_RH + 4 * 18432;
constexpr int LDS_BYTES = 163840;
static_assert(L_Q + 128 * 88 <= L_HALO && L_KV + 144 * 168 <= L_MISC && L_XO + 8192 <= L_MISC, "lds");
constexpr int CTRL = 7;
constexpr unsigned SENT = 0xffffffffu;
constexpr unsigned SPIN_CAP = 1u << 18;

template <int I, int N, class Fn>
__device__ __forceinline__ void sfor(Fn&& f) {
  if constexpr (I < N) {
    f(std::integral_constant<int, I>{});
    sfor<I + 1, N>(f);
  }
}

template <int OFF, bool SC1>
__device__ __forceinline__ void dma_s(unsigned voff, const void* sbase, unsigned ldsaddr) {
  if constexpr (SC1)
    asm volatile("s_mov_b32 m0, %2\n\ts_nop 0\n\tglobal_load_lds_dwordx4 %0, %1 offset:%3 sc1" ::"v"(voff), "s"(sbase), "s"(ldsaddr), "n"(OFF) : "memory");
  else
    asm volatile("s_mov_b32 m0, %2\n\ts_nop 0\n\tglobal_load_lds_dwordx4 %0, %1 offset:%3" ::"v"(voff), "s"(sbase), "s"(ldsaddr), "n"(OFF) : "memory");
}
__device__ __forceinline__ void dma_v(const void* vptr, unsigned ldsaddr) {
  asm volatile("s_mov_b32 m0, %1\n\ts_nop 0\n\tglobal_load_lds_dwordx4 %0, off" ::"v"(vptr), "s"(ldsaddr) : "memory");
}
__device__ __forceinline__ void dma_v_nt(const void* vptr, unsigned ldsaddr) {
  asm volatile("s_mov_b32 m0, %1\n\ts_nop 0\n\tglobal_load_lds_dwordx4 %0, off nt" ::"v"(vptr), "s"(ldsaddr) : "memory");
}
__device__ __forceinline__ void dma_s_nt(unsigned voff, const void* sbase, unsigned ldsaddr) {
  asm volatile("s_mov_b32 m0, %2\n\ts_nop 0\n\tglobal_load_lds_dwordx4 %0, %1 nt" ::"v"(voff), "s"(sbase), "s"(ldsaddr) : "memory");
}
template <int OFF>
__device__ __forceinline__ void st16_sc1(unsigned voff, const void* sbase, v4u d) {
  asm volatile("global_store_dwordx4 %0, %1, %2 offset:%3 sc1\n\ts_nop 1" ::"v"(voff), "v"(d), "s"(sbase), "n"(OFF) : "memory");
}
__device__ __forceinline__ void st4_sc1(unsigned voff, const void* sbase, unsigned d) {
  asm volatile("global_store_dword %0, %1, %2 sc1" ::"v"(voff), "v"(d), "s"(sbase) : "memory");
}
template <int OFF>
__device__ __forceinline__ void ld16_sc1(v4u& d, unsigned voff, const void* sbase) {
  asm volatile("global_load_dwordx4 %0, %1, %2 offset:%3 sc1" : "=v"(d) : "v"(voff), "s"(sbase), "n"(OFF) : "memory");
}
__device__ __forceinline__ unsigned ld4_sc1_wait(unsigned voff, const void* sbase) {
  unsigned d;
  asm volatile("global_load_dword %0, %1, %2 sc1\n\ts_waitcnt vmcnt(0)" : "=&v"(d) : "v"(voff), "s"(sbase) : "memory");
  return d;
}
__device__ __forceinline__ void ld4_sc1(unsigned& d, unsigned voff, const void* sbase) {
  asm volatile("global_load_dword %0, %1, %2 sc1" : "=v"(d) : "v"(voff), "s"(sbase) : "memory");
}
template <int N>
__device__ __forceinline__ void wait_vm_pin(unsigned& d) { asm volatile("s_waitcnt vmcnt(%1)" : "+v"(d) : "n"(N) : "memory"); }
template <int N>
__device__ __forceinline__ void wait_bar() {
  static_assert(N >= 0 && N <= 63, "vmcnt");
  asm volatile("s_waitcnt vmcnt(%0) lgkmcnt(0)\n\ts_barrier" ::"n"(N) : "memory");
}
__device__ __forceinline__ void lds_bar() { asm volatile("s_waitcnt lgkmcnt(0)\n\ts_barrier" ::: "memory"); }
template <int N>
__device__ __forceinline__ void wait_vm() {
  static_assert(N >= 0 && N <= 63, "vmcnt");
  asm volatile("s_waitcnt vmcnt(%0)" ::"n"(N) : "memory");
}
template <class T>
__device__ __forceinline__ void pin4(T& a, T& b, T& c, T& d) { asm volatile("" : "+v"(a), "+v"(b), "+v"(c), "+v"(d)); }
template <class T>
__device__ __forceinline__ void pin5(T& a, T& b, T& c, T& d, T& e) { asm volatile("" : "+v"(a), "+v"(b), "+v"(c), "+v"(d), "+v"(e)); }

__device__ __forceinline__ v4u lds16(ldsp p, int off) { return *(const LAS v4u*)(p + off); }
__device__ __forceinline__ v2u lds8(ldsp p, int off) { return *(const LAS v2u*)(p + off); }
__device__ __forceinline__ v2u ldstr(ldsp p, int off) {
  return __builtin_bit_cast(v2u, __builtin_amdgcn_ds_read_tr16_b64_v4i16((LAS s16x4*)(p + off)));
}
__device__ __forceinline__ f32x4 mfma(v4u a, v4u b, f32x4 c) {
  return __builtin_amdgcn_mfma_f32_16x16x32_bf16(__builtin_bit_cast(bf16x8, a), __builtin_bit_cast(bf16x8, b), c, 0, 0, 0);
}
__device__ __forceinline__ unsigned pk2(float a, float b) {
  f32x2 v = {a, b};
  return __builtin_bit_cast(unsigned, __builtin_convertvector(v, bf16x2));
}
__device__ __forceinline__ float bflo(unsigned u) { return __uint_as_float(u << 16); }
__device__ __forceinline__ float bfhi(unsigned u) { return __uint_as_float(u & 0xffff0000u); }
__device__ __forceinline__ float rbf(float a) { return bflo(pk2(a, a)); }
__device__ __forceinline__ unsigned gelu2(f32x2 x) {
  const f32x2 c0 = {-2.3022081983f, -2.3022081983f}, c1 = {-0.1029432397f, -0.1029432397f};
  f32x2 z = x * (x * x * c1 + c0);
  f32x2 e = {__builtin_amdgcn_exp2f(z[0]), __builtin_amdgcn_exp2f(z[1])};
  e = e + 1.0f;
  f32x2 r = {__builtin_amdgcn_rcpf(e[0]), __builtin_amdgcn_rcpf(e[1])};
  f32x2 y = x * r;
  return pk2(y[0], y[1]);
}
#define SB() __builtin_amdgcn_sched_barrier(0)
__device__ __forceinline__ int fresh_lane() {
  unsigned z = 0;
  asm volatile("" : "+v"(z));
  return (int)__builtin_amdgcn_mbcnt_hi(~0u, __builtin_amdgcn_mbcnt_lo(~0u, z));
}
__device__ __forceinline__ int opaque(int v) {
  asm volatile("" : "+v"(v));
  return v;
}
enum OpT { NONE = 0, XA, HALO, PQS, P2S, PB, BV, A2, POLLP, POLLR, NOPT };
struct Op {
  int t, a, b;
};
constexpr int PQSS = 1, P2SS = 2;
constexpr int JP = 4;
constexpr int PP1 = 6;
constexpr int JH = 2;
constexpr int KPH = 13, KC = 16;
constexpr int NBV = 3, BV0 = 4;
constexpr int NAW = 3;
constexpr int NH = 6;
constexpr int NSL = 6;
struct Prog {
  Op a[20][NSL], b[21][NSL], c[9][4][3];
  int wa[10], wp1, wx, wb[5], wph, wpo, w2[9];
};
constexpr int imax(int a, int b) { return a > b ? a : b; }
constexpr Prog make_prog(int hf) {
  Prog s{};
  const int nA = hf == 0 ? 4 : 3, NK = hf == 0 ? 9 : 8;
  int fa[20] = {}, fb[21] = {};
  auto pa = [&](int kk, int t, int x, int y) { s.a[kk][fa[kk]++] = Op{t, x, y}; };
  auto pb = [&](int kk, int t, int x, int y) { s.b[kk][fb[kk]++] = Op{t, x, y}; };
  for (int q = 0; q < nA; ++q) pa(0, XA, 1, q);
  for (int q = 0; q < nA; ++q) pa(1, XA, 2, q);
  pa(2 * PQSS, PQS, 0, 0); pa(2 * PQSS, PQS, 1, 0);
  pa(2 * P2SS, P2S, 0, 0); pa(2 * P2SS, P2S, 1, 0);
  if (hf == 1) pa(2 * PP1, POLLP, 0, 0);
  for (int i = 1; i <= 7; ++i)
    for (int q = 0; q < nA; ++q) pa(2 * i + (q >> 1), XA, i + 2, q);
  if (hf == 1) pa(16, HALO, 0, 0);
  for (int q = 0; q < 4; ++q) pa(16 + q, PB, 0, q);
  if (hf == 1) pb(KPH, POLLR, 0, 0);
  for (int j = 0; j <= 2; ++j) {
    if (hf == 1) pb(4 * j + 2, HALO, j + 2, 0);
    for (int q = 0; q < 4; ++q) pb(4 * j + 2 + (q >> 1), PB, j + 2, q);
  }
  for (int f = 0; f < 5 * NBV; ++f) pb(BV0 + f, BV, f, 0);
  for (int q = 0; q < 4; ++q) pb(19, A2, 0, q);
  for (int i = 0; i < NK; ++i) {
    int f[4] = {};
    if (i + NBV < NK)
      for (int k = 0; k < 5; ++k) { const int m = k < 4 ? k : 1; s.c[i][m][f[m]++] = Op{BV, 5 * (i + NBV) + k, 0}; }
    if (i + NAW < NH)
      for (int m = 0; m < 4; ++m) s.c[i][m][f[m]++] = Op{A2, i + NAW, m};
  }
  int n = 0, last[NOPT][10] = {}, np = 0;
  auto run = [&](const Op* o, int cnt) {
    for (int k = 0; k < cnt; ++k) {
      if (o[k].t == NONE) continue;
      if (o[k].t == POLLP || o[k].t == POLLR) { np = n; continue; }
      n += 1;
      last[o[k].t][o[k].t == BV ? o[k].a / 5 : (o[k].t == XA || o[k].t == PB || o[k].t == A2) ? o[k].a : 0] = n;
    }
  };
  n += nA + 1; last[XA][0] = n;
  n += 4;
  for (int kk = 0; kk < 20; ++kk) {
    const int i = kk >> 1;
    if ((kk & 1) == 0) {
      if (i == 8) s.wp1 = n - np;
      s.wa[i] = n - (i == JP ? imax(last[XA][i], imax(last[P2S][0], last[PQS][0])) : last[XA][i]);
    }
    run(s.a[kk], NSL);
  }
  s.wx = n - last[PB][0];
  n += 2 + hf + 4; last[PB][1] = n;
  n += 2 + (hf == 0 ? 2 : 0);
  const int lastH = n;
  for (int kk = 0; kk <= 20; ++kk) {
    const int j = kk >> 2;
    if (kk == KC) s.wph = n - np;
    if ((kk & 3) == 0 && kk > 0 && kk < 20) s.wb[j] = n - (j == JH ? imax(last[PB][j], lastH) : last[PB][j]);
    run(s.b[kk], NSL);
  }
  n += 4; last[A2][1] = n;
  n += 2;
  n += 4; last[A2][2] = n;
  for (int i = 0; i < NK; ++i) {
    if (i == NH) {
      s.wpo = n - np;
      for (int k = NH; k < NK; ++k) { n += 4; last[A2][k] = n; }
    }
    if (last[A2][i] == 0 || last[BV][i] == 0) throw 1;
    s.w2[i] = n - imax(last[A2][i], last[BV][i]);
    if (i == NH - 1) np = n;
    for (int m = 0; m < 4; ++m) run(s.c[i][m], 3);
  }
  return s;
}
template <int HF>
struct PG {
  static constexpr Prog v = make_prog(HF);
};

struct Params {
  const u16* x;
  const u16* wqkv;
  const u16* wo;
  const u16* w1;
  const u16* b1;
  const u16* w2;
  const u16* b2;
  const u16* gw;
  u16* out;
  char* ws;
};

__device__ __forceinline__ void poll_mail(const char* ws, unsigned mb_off, unsigned ack_off, bool ack_on) {
  for (unsigned spin = 0; spin < SPIN_CAP; ++spin) {
    if (__all(ld4_sc1_wait(mb_off, ws) == 1u)) break;
    if (ack_on) st4_sc1(ack_off, ws, 1u);
    __builtin_amdgcn_s_sleep(2);
  }
}

#define SC PG<HF>::v
template <int N>
using IC = std::integral_constant<int, N>;

template <int HF>
__device__ __forceinline__ void body(const Params& p, ldsp lds, const int w, int lane, const int rb, const int cg) {
  constexpr bool OW = HF == 0;
  const int s = w & 3;
  int r16 = lane & 15, g = lane >> 4;
  const int ct = cg & 7, hf2 = cg >> 3;
  const char* ws = p.ws;
  const unsigned ldsb = (unsigned)(size_t)lds;
  unsigned l16 = lane * 16;
  auto relane = [&]() { lane = fresh_lane(); r16 = lane & 15; g = lane >> 4; l16 = lane * 16; };
  const int rot = (20 - rb + 3 * cg) % 10;
  auto se = [&](int st) { const int t = st + rot; return t >= 10 ? t - 10 : t; };
  const unsigned xo0 = (lane >> 3) * 1280 + (((lane & 7) ^ (lane >> 4)) * 16);
  const unsigned xo1 = (8 + (lane >> 3)) * 1280 + (((lane & 7) ^ (4 + (lane >> 4))) * 16);
  const char* xb = (const char*)p.x + (size_t)(rb * 128 + 16 * w) * 1280;
  unsigned wo_[2];
  sfor<0, 2 - HF>([&](auto I) {
    const int P = HF == 0 ? s + 4 * I : 8 + s;
    const int Q = 64 * P + lane, rowp = Q / 12, pos = Q % 12;
    const int c = (pos >> 1) ^ ((rowp >> 2) & 1);
    const int r = (rowp & 0x33) | ((rowp & 4) << 1) | ((rowp & 8) >> 1);
    wo_[I] = r * 3072 + c * 32 + (pos & 1) * 16;
  });
  const char* w1b = (const char*)p.w1 + cg * 192;
  auto issueA = [&](int st, int part) {
    const unsigned sl = ldsb + (st & 3) * L_SLOTA;
    const int e = se(st);
    if (part == 0) dma_s<0, false>(xo0, xb + e * 128, sl + w * 2048);
    if (part == 1) dma_s<0, false>(xo1, xb + e * 128, sl + w * 2048 + 1024);
    if (part == 2) dma_s<0, false>(wo_[0], w1b + e * 196608, sl + 16384 + (HF == 0 ? s : 8 + s) * 1024);
    if constexpr (HF == 0)
      if (part == 3) dma_s<0, false>(wo_[1], w1b + e * 196608, sl + 16384 + (s + 4) * 1024);
  };
  issueA(0, 0); issueA(0, 1); issueA(0, 2); issueA(0, 3);
  const unsigned myL = rb * 16 + cg;
  auto mbp = [&](int l) -> unsigned { return WS_MB + myL * 256 + (l & 31) * 4; };
  auto ackp = [&](int l) -> unsigned { return WS_MB + (((l & 31) >> 1) * 16 + ct + 8 * (l & 1)) * 256 + (rb * 2 + hf2) * 4; };
  auto mbr = [&](int l) -> unsigned { return WS_MB + myL * 256 + (32 + (l & 15)) * 4; };
  auto ackr = [&](int l) -> unsigned { return WS_MB + (rb * 16 + (l & 15)) * 256 + (32 + cg) * 4; };
  if (HF == 1 && w == CTRL) {
    if (lane < 48) st4_sc1(WS_MB + myL * 256 + lane * 4, ws, 0u);
    if (lane == 0) *(LAS unsigned*)(lds + L_CTL) = 0u;
  }
  {
    const int c = 16 * w + r16;
    const u16* src = c < 80 ? p.gw + c * 8 : c < 92 ? p.b1 + cg * 96 + (c - 80) * 8 : c < 102 ? p.b2 + ct * 80 + (c - 92) * 8 : p.gw;
    if (lane < 16) dma_v(src, ldsb + L_MISC + w * 256);
  }
  constexpr int S2 = L_SCR + 9728;
  {
    const char* src = (const char*)p.wqkv + (size_t)(40 * rb + w) * 3840 + cg * 80;
    sfor<0, 2>([&](auto I) {
      const int c = I * 64 + lane, gi = c / 15, ch = c % 15, seg = ch / 5;
      const unsigned o = gi * 30720 + (seg == 2 ? 0 : 1280 + seg * 1280) + (ch % 5) * 16;
      if (I == 0 || lane < 11) dma_s_nt(o, src, ldsb + L_SCR + w * 1200 + I * 1024);
    });
  }
  const int jme = rb * 2 + hf2;
  const int G0 = 17 * (jme >> 1) + 8 * (jme & 1) + w, G8 = 17 * (jme >> 1) + 16;
  const int ntx = (jme & 1) ? 3 + (w & 1) : (w < 3 ? w : 0);
  auto p2src = [&](int G, int row) {
    const int t = G >> 2, gq = G & 3;
    const int S = G - 192;
    return (t < 48 ? p.w2 + (32 * t + 16 * (row >> 2) + 4 * gq + (row & 3)) * 640 : p.wo + (40 * (S / 5) + 4 * (S % 5) + 20 * (row >> 2) + (row & 3)) * 640) + ct * 80;
  };
  auto p2dst = [&](int G) -> unsigned { return WS_P2 + ct * 348160 + (G >> 2) * 5120 + (G & 3) * 256; };
  sfor<0, 2>([&](auto J) {
    const int c = J * 64 + lane;
    const u16* src = p2src(G0, c / 10) + (c % 10) * 8;
    if constexpr (J == 1) {
      if (lane >= 16) src = p2src(G8, ((lane - 16) >> 1) & 7) + ntx * 16 + (lane & 1) * 8;
    }
    if (J == 0 || lane < 32) dma_v_nt(src, ldsb + S2 + w * 1536 + J * 1024);
  });
  int pla[2];
  unsigned pds[2];
  sfor<0, 2>([&](auto J) {
    const int gi = J == 0 ? g : 4, Gq = 5 * rb + gi;
    pla[J] = opaque(L_SCR + (r16 >> 2) * 1200 + gi * 240 + w * 32 + (lane & 3) * 8);
    pds[J] = opaque(WS_PQ + cg * 163840 + (Gq >> 2) * 8192 + w * 1024 + ((Gq & 3) * 16 + r16) * 16);
  });
  auto issueH = [&](int jb) {
    const int l = fresh_lane();
    const int hrow = rb * 128 - 16 + 8 * (s & 1) + (l >> 3);
    const unsigned ho = (hrow > 0 ? hrow : 0) * 1280 + (((l & 7) ^ (4 * (s & 1) + (l >> 4))) * 16);
    const int st = 2 * jb + (s >> 1);
    dma_s<0, false>(ho, (const char*)p.x + se(st) * 128, ldsb + L_HALO + st * 2048 + (s & 1) * 1024);
  };
  const char* pqb = ws + WS_PQ + cg * 163840 + w * 2048;
  auto issueB = [&](int jb, int q) {
    dma_s<0, true>(l16, pqb + se(2 * jb + (q >> 1)) * 16384 + (q & 1) * 1024, ldsb + ((jb + 2) % 3) * L_SLOTB + (q >> 1) * 16384 + w * 2048 + (q & 1) * 1024);
  };

  const int xa = (32 * s + r16) * 128 + ((g ^ (r16 >> 1)) * 16);
  const int q4 = r16 >> 2, p4 = lane & 3;
  const int wa = 16384 + 192 * (16 * (g >> 1) + 4 * (g & 1) + q4) + 8 * p4;
  constexpr int HI = 2 * L_SLOTA;
  const int xab[2][2] = {{opaque(xa), opaque(xa ^ 64)}, {opaque(xa + HI), opaque((xa ^ 64) + HI)}};
  const int wab[2][2] = {{opaque(wa + 32 * (g & 1)), opaque(wa - 32 * (g & 1))}, {opaque(wa + 32 * (g & 1) + HI), opaque(wa - 32 * (g & 1) + HI)}};

  v4u xf[2][20];
  constexpr int NA = HF == 0 ? 4 : 2;
  f32x4 acc[2][NA];
  unsigned pollv = 0;
  {
    v4u bf[2][NA];
    sfor<0, 21>([&](auto KK) {
      constexpr int kk = KK, i = kk >> 1, ks = kk & 1;
      constexpr int sl = (i & 1) * L_SLOTA, hb = (i >> 1) & 1;
      auto rdx = [&](auto M) {
        if constexpr (kk < 20) xf[M][kk] = lds16(lds, sl + M * 2048 + xab[hb][ks]);
      };
      auto rdb = [&](auto N) {
        if constexpr (kk < 20) {
          constexpr int c = HF == 0 ? (int)N : 4 + N;
          const int a0 = sl + wab[hb][c & 1] + 192 * 32 * ks + 32 * c;
          const v2u lo = ldstr(lds, a0), hi = ldstr(lds, a0 + 192 * 8);
          bf[ks][N] = v4u{lo[0], lo[1], hi[0], hi[1]};
        }
      };
      auto mm = [&](auto N, auto M) {
        if constexpr (kk >= 1) acc[M][N] = mfma(bf[(kk - 1) & 1][N], xf[M][kk - 1], acc[M][N]);
      };
      auto vm = [&](auto Q) {
        if constexpr (kk < 20) {
          constexpr Op o = SC.a[kk][Q];
          if constexpr (o.t == XA) issueA(o.a, o.b);
          if constexpr (o.t == PB) issueB(o.a, o.b);
          if constexpr (o.t == POLLP) ld4_sc1(pollv, mbp(fresh_lane()), ws);
          if constexpr (o.t == HALO) issueH(o.a);
          if constexpr (o.t == PQS) {
            const v2u lo = ldstr(lds, pla[o.a]), hi = ldstr(lds, pla[o.a] + 4800);
            v4u d = v4u{lo[0], lo[1], hi[0], hi[1]};
            if constexpr (HF == 1) {
              const v4u gv = lds16(lds, L_GW + (5 * rb + (o.a == 0 ? g : 4)) * 16);
              if (w == 7 && r16 == 8) d = gv;
            }
            if (o.a == 0 || lane < 16) st16_sc1<0>(pds[o.a], ws, d);
          }
          if constexpr (o.t == P2S) {
            const int b0 = S2 + w * 1536 + (lane & 3) * 8;
            if constexpr (o.a == 0) {
              const int la = b0 + (r16 >> 2) * 160 + g * 32;
              const v2u lo = ldstr(lds, la), hi = ldstr(lds, la + 640);
              st16_sc1<0>(p2dst(G0) + g * 1024 + r16 * 16, ws, v4u{lo[0], lo[1], hi[0], hi[1]});
            } else {
              const int la = g == 0 ? b0 + (r16 >> 2) * 160 + 128 : b0 + 1280 + (r16 >> 2) * 32;
              const v2u lo = ldstr(lds, la), hi = ldstr(lds, la + (g == 0 ? 640 : 128));
              const unsigned d1 = (g == 0 ? p2dst(G0) + 4096 : p2dst(G8) + ntx * 1024) + r16 * 16;
              if (lane < 32) st16_sc1<0>(d1, ws, v4u{lo[0], lo[1], hi[0], hi[1]});
            }
          }
        }
      };
      if constexpr (kk < 20 && ks == 0) {
        if constexpr (HF == 1 && i == 8) {
          wait_vm_pin<SC.wp1>(pollv);
          if (w == CTRL) {
            const int l = fresh_lane();
            if (!__all(pollv == 1u)) poll_mail(ws, mbp(l), ackp(l), l < 32);
            if (l < 32) st4_sc1(ackp(l), ws, 1u);
          }
        }
        wait_bar<SC.wa[i]>();
        if constexpr (i == 0) {
          sfor<0, NA>([&](auto N) {
            const v2u bb = lds8(lds, L_B1 + ((HF == 0 ? 0 : 4) + N) * 32 + g * 8);
            acc[0][N] = acc[1][N] = f32x4{bflo(bb[0]), bfhi(bb[0]), bflo(bb[1]), bfhi(bb[1])};
          });
        }
        if constexpr (HF == 1) {
          if constexpr (i == JP) {
            if (w == CTRL) {
              const int l = fresh_lane();
              if (l < 32) st4_sc1(ackp(l), ws, 1u);
            }
          }
        }
        SB();
      }
      if constexpr (HF == 0) {
        rdx(IC<0>{}); mm(IC<0>{}, IC<0>{}); SB();
        vm(IC<0>{}); rdx(IC<1>{}); mm(IC<0>{}, IC<1>{}); SB();
        vm(IC<1>{}); rdb(IC<0>{}); mm(IC<1>{}, IC<0>{}); SB();
        rdb(IC<1>{}); mm(IC<1>{}, IC<1>{}); SB();
        vm(IC<2>{}); rdb(IC<2>{}); mm(IC<2>{}, IC<0>{}); SB();
        rdb(IC<3>{}); mm(IC<2>{}, IC<1>{}); SB();
        vm(IC<3>{}); mm(IC<3>{}, IC<0>{}); SB();
        vm(IC<4>{}); mm(IC<3>{}, IC<1>{}); SB();
        vm(IC<5>{}); SB();
      } else {
        rdx(IC<0>{}); rdx(IC<1>{}); mm(IC<0>{}, IC<0>{}); SB();
        rdb(IC<0>{}); rdb(IC<1>{}); mm(IC<0>{}, IC<1>{}); SB();
        vm(IC<0>{}); mm(IC<1>{}, IC<0>{}); SB();
        vm(IC<1>{}); mm(IC<1>{}, IC<1>{}); SB();
        vm(IC<2>{}); vm(IC<3>{}); vm(IC<4>{}); vm(IC<5>{}); SB();
      }
    });
  }
  wait_bar<SC.wx>();
  relane();
  const char* ob = ws + WS_HA + (size_t)((8 * rb + w) * 68 + 48) * 1024 + cg * 1280;
  {
    const char* hab = ws + WS_HA + (size_t)((8 * rb + 2 * s) * 68 + 3 * cg + 2 * HF) * 1024;
    sfor<0, NA>([&](auto Fq) {
      constexpr int M = Fq / (NA / 2), T = Fq % (NA / 2);
      v4u o;
      sfor<0, 2>([&](auto Hh) {
        const f32x4 a = acc[M][2 * T + Hh];
        o[2 * Hh] = gelu2(f32x2{a[0], a[1]});
        o[2 * Hh + 1] = gelu2(f32x2{a[2], a[3]});
      });
      st16_sc1<T * 1024>(l16, hab + M * 69632, o);
      if constexpr (Fq == 0) {
        if constexpr (HF == 1) issueH(1);
        issueB(1, 0); issueB(1, 1);
      }
      if constexpr (Fq == 1) { issueB(1, 2); issueB(1, 3); }
      if constexpr (Fq == NA - 2 + HF) st16_sc1<0>(l16, ob, v4u{SENT, SENT, SENT, SENT});
      if constexpr (Fq == NA - 1) {
        if (lane < 16) st16_sc1<1024>(l16, ob, v4u{SENT, SENT, SENT, SENT});
      }
      SB();
    });
  }
  constexpr int NK = OW ? 9 : 8, NO = NK - NH;
  const int r6 = (rb + (cg & 3) * 3 / 2) % 6, ro = (rb + cg) % NO;
  auto kk2 = [&](int i) {
    if (i < NH) { const int t = i + r6; return 6 * w + (t >= 6 ? t - 6 : t); }
    const int t = i - NH + ro;
    return 48 + 5 * s + (OW ? 0 : 3) + (t >= NO ? t - NO : t);
  };
  const char* bb2 = ws + (unsigned)__builtin_amdgcn_readfirstlane((int)(WS_P2 + ct * 348160 + 2048));
  v4u bv[NK][5];
  auto issueBv = [&](auto Fq) {
    constexpr int f = Fq;
    ld16_sc1<(f % 5) * 1024 - 2048>(bv[f / 5][f % 5], l16, bb2 + kk2(f / 5) * 5120);
  };
  const char* ab = ws + WS_HA + (size_t)(8 * rb + 4 * hf2) * 69632;
  v4u af[NK][4];
  auto issueA2m = [&](auto I, auto M) { ld16_sc1<0>(af[I][M], l16, ab + kk2(I) * 1024 + M * 69632); };
  auto issueA2 = [&](auto I) { sfor<0, 4>([&](auto M) { issueA2m(I, M); }); };
  constexpr int NB = HF == 0 ? 3 : 5;
  f32x4 ac2[2][NB];
  f32x4 ach[2];
  sfor<0, 2>([&](auto M) {
    sfor<0, NB>([&](auto N) { ac2[M][N] = f32x4{0, 0, 0, 0}; });
    ach[M] = f32x4{0, 0, 0, 0};
  });
  {
    const int ha0 = L_HALO + r16 * 128 + ((g ^ (r16 >> 1)) * 16);
    const int hab[2] = {opaque(ha0), opaque(ha0 ^ 64)};
    const int lb[2] = {(int)l16, opaque(l16 + 2 * L_SLOTB)};
    const int lbs[2] = {opaque(l16 + s * 1024), opaque(l16 + s * 1024 + 2 * L_SLOTB)};
    v4u bf[2][NB];
    v4u hq[3];
    sfor<0, 21>([&](auto KK) {
      constexpr int kk = KK, j = kk >> 2, ks = kk & 1;
      constexpr int slot = (j + 2) % 3, hb = slot >> 1, sl = (slot & 1) * L_SLOTB + (kk & 3) * 8192;
      auto rdb = [&](auto N) {
        if constexpr (kk < 20) bf[ks][N] = lds16(lds, sl + ((HF == 0 ? 5 : 0) + N) * 1024 + lb[hb]);
      };
      auto rdh = [&](auto N) {
        if constexpr (kk < 20) {
          if constexpr (N == 0) hq[0] = lds16(lds, (kk >> 1) * 2048 + hab[ks]);
          if constexpr (N == 1) hq[1] = lds16(lds, sl + lbs[hb]);
          if constexpr (N == 2) hq[2] = lds16(lds, sl + 4096 + lb[hb]);
        }
      };
      auto mm = [&](auto N, auto M) {
        if constexpr (kk >= 1) ac2[M][N] = mfma(bf[(kk - 1) & 1][N], xf[M][kk - 1], ac2[M][N]);
      };
      auto mh = [&](auto M) {
        if constexpr (kk < 20) ach[M] = mfma(hq[1 + M], hq[0], ach[M]);
      };
      auto vm = [&](auto Q) {
        constexpr Op o = SC.b[kk][Q];
        if constexpr (o.t == PB) issueB(o.a, o.b);
        if constexpr (o.t == BV) issueBv(IC<o.a>{});
        if constexpr (o.t == A2) issueA2m(IC<o.a>{}, IC<o.b>{});
        if constexpr (o.t == HALO) issueH(o.a);
        if constexpr (o.t == POLLR) ld4_sc1(pollv, mbr(fresh_lane()), ws);
      };
      if constexpr (HF == 1 && kk == KC) {
        wait_vm_pin<SC.wph>(pollv);
        if (w == CTRL) {
          const int l = fresh_lane();
          if (!__all(pollv == 1u)) poll_mail(ws, mbr(l), ackr(l), l < 16);
          if (l < 16) st4_sc1(ackr(l), ws, 1u);
        }
      }
      if constexpr ((kk & 3) == 0 && kk > 0 && kk < 20) {
        wait_bar<SC.wb[j]>();
        if constexpr (j == JH && HF == 1) {
          if (w == CTRL) {
            const int l = fresh_lane();
            if (l < 16) st4_sc1(ackr(l), ws, 1u);
          }
        }
        SB();
      }
      if constexpr (HF == 0) {
        rdh(IC<0>{}); rdh(IC<1>{}); mm(IC<0>{}, IC<0>{}); SB();
        vm(IC<0>{}); rdh(IC<2>{}); mm(IC<0>{}, IC<1>{}); SB();
        rdb(IC<0>{}); mm(IC<1>{}, IC<0>{}); SB();
        vm(IC<1>{}); mm(IC<1>{}, IC<1>{}); SB();
        rdb(IC<1>{}); mm(IC<2>{}, IC<0>{}); SB();
        vm(IC<2>{}); rdb(IC<2>{}); mm(IC<2>{}, IC<1>{}); SB();
        vm(IC<3>{}); mh(IC<0>{}); SB();
        vm(IC<4>{}); mh(IC<1>{}); SB();
        vm(IC<5>{}); SB();
      } else {
        rdb(IC<0>{}); mm(IC<0>{}, IC<0>{}); SB();
        vm(IC<0>{}); mm(IC<0>{}, IC<1>{}); SB();
        rdb(IC<1>{}); mm(IC<1>{}, IC<0>{}); SB();
        vm(IC<1>{}); mm(IC<1>{}, IC<1>{}); SB();
        rdb(IC<2>{}); mm(IC<2>{}, IC<0>{}); SB();
        vm(IC<2>{}); mm(IC<2>{}, IC<1>{}); SB();
        rdb(IC<3>{}); mm(IC<3>{}, IC<0>{}); SB();
        vm(IC<3>{}); mm(IC<3>{}, IC<1>{}); SB();
        rdb(IC<4>{}); mm(IC<4>{}, IC<0>{}); SB();
        vm(IC<4>{}); mm(IC<4>{}, IC<1>{}); SB();
        vm(IC<5>{}); SB();
      }
    });
  }
  relane();
  {
    auto put = [&](int off, f32x4 a) { *(LAS v2u*)(lds + off) = v2u{pk2(a[0], a[1]), pk2(a[2], a[3])}; };
    if constexpr (HF == 1) {
      sfor<0, 2>([&](auto M) {
        sfor<0, 5>([&](auto N) { put(L_KV + (16 + 32 * s + 16 * M + r16) * 168 + 32 * N + 8 * g, ac2[M][N]); });
      });
    } else {
      sfor<0, 2>([&](auto M) {
        sfor<0, 2>([&](auto N) { put(L_Q + (32 * s + 16 * M + r16) * 88 + 32 * N + 8 * g, ac2[M][N]); });
        if (g < 2) put(L_Q + (32 * s + 16 * M + r16) * 88 + 64 + 8 * g, ac2[M][2]);
        const float t = rbf(ac2[M][2][0]);
        const float gg = rbf(__builtin_amdgcn_rcpf(1.0f + __builtin_amdgcn_exp2f(-1.4426950409f * t)));
        if (g == 2) *(LAS f32x2*)(lds + L_G + (32 * s + 16 * M + r16) * 8) = f32x2{gg, rbf(1.0f - gg)};
      });
      put(L_KV + r16 * 168 + 32 * s + 8 * g, ach[0]);
      if (s == 0) put(L_KV + r16 * 168 + 128 + 8 * g, ach[1]);
    }
  }
  const int R0 = 16 * w;
  const int kb = opaque(L_KV + (R0 + r16) * 168 + g * 16), qb = opaque(L_Q + (R0 + r16) * 88 + g * 16);
  const int vb = opaque(L_KV + (R0 + 4 * g + (r16 >> 2)) * 168 + 80 + (lane & 3) * 8);
  const int gb = opaque(L_G + (R0 + r16) * 8);
  const unsigned m01 = opaque(g < 3 ? ~0 : 0), m23 = opaque(g < 2 ? ~0 : 0);
  bool inw[4];
  sfor<0, 4>([&](auto C) {
    const int dl = (C < 2 ? 16 : 0) + (r16 & 14) - 4 * g - 2 * (C & 1);
    inw[C] = dl >= 0 && dl <= 14 && rb * 128 + R0 + r16 - dl >= 0;
  });
  SB();
  lds_bar();
  {
    issueA2(IC<1>{});
    const bool par = lane & 1;
    auto ld2 = [&](int a) { const v2u x = lds8(lds, a), y = lds8(lds, a + 8); return v4u{x[0], x[1], y[0], y[1]}; };
    v4u q[2], kf[2][2], vf[2][2];
    sfor<0, 2>([&](auto H) {
      q[H] = ld2(qb + H * 40);
      sfor<0, 2>([&](auto T) { kf[H][T] = ld2(kb + H * 40 + T * 16 * 168); });
    });
    const float gate = *(const LAS float*)(lds + gb);
    sfor<0, 2>([&](auto H) {
      sfor<0, 2>([&](auto Dt) {
        const v2u lo = ldstr(lds, vb + H * 40 + Dt * 32), hi = ldstr(lds, vb + H * 40 + Dt * 32 + 16 * 168);
        vf[H][Dt] = v4u{lo[0], lo[1], hi[0], hi[1]};
      });
    });
    SB();
    f32x4 st[2][2];
    sfor<0, 2>([&](auto H) {
      q[H][0] &= m01; q[H][1] &= m01; q[H][2] &= m23; q[H][3] &= m23;
      sfor<0, 2>([&](auto T) { st[H][T] = mfma(kf[H][T], q[H], f32x4{0, 0, 0, 0}); });
    });
    float lg[2][4], mx[2], e[2][4], sm[2];
    sfor<0, 2>([&](auto H) {
      sfor<0, 4>([&](auto C) {
        const f32x4 t = st[H][C >> 1];
        const float v = (par ? t[2 * (C & 1) + 1] : t[2 * (C & 1)]) * (0.22360679774997896f * 1.4426950408889634f);
        lg[H][C] = inw[C] ? v : -1e9f * 1.4426950408889634f;
      });
      mx[H] = fmaxf(fmaxf(lg[H][0], lg[H][1]), fmaxf(lg[H][2], lg[H][3]));
    });
    auto xred2 = [&](float* v, auto op) {
      auto r = __builtin_amdgcn_permlane32_swap(__float_as_uint(v[0]), __float_as_uint(v[1]), false, false);
      float t = op(__uint_as_float(r[0]), __uint_as_float(r[1]));
      auto u = __builtin_amdgcn_permlane16_swap(__float_as_uint(t), __float_as_uint(t), false, false);
      t = op(__uint_as_float(u[0]), __uint_as_float(u[1]));
      r = __builtin_amdgcn_permlane32_swap(__float_as_uint(t), __float_as_uint(t), false, false);
      v[0] = __uint_as_float(r[0]);
      v[1] = __uint_as_float(r[1]);
    };
    xred2(mx, [](float x, float y) { return fmaxf(x, y); });
    sfor<0, 2>([&](auto H) {
      sfor<0, 4>([&](auto C) { e[H][C] = __builtin_amdgcn_exp2f(lg[H][C] - mx[H]); });
      sm[H] = (e[H][0] + e[H][1]) + (e[H][2] + e[H][3]);
    });
    xred2(sm, [](float x, float y) { return x + y; });
    f32x4 ot[2][2];
    sfor<0, 2>([&](auto H) {
      const float inv = gate * __builtin_amdgcn_rcpf(sm[H]);
      const f32x2 wv = {par ? 0.f : inv, par ? inv : 0.f};
      v4u pf;
      sfor<0, 4>([&](auto C) {
        const f32x2 t = wv * e[H][C];
        pf[(int)C] = pk2(t[0], t[1]);
      });
      sfor<0, 2>([&](auto Dt) { ot[H][Dt] = mfma(vf[H][Dt], pf, f32x4{0, 0, 0, 0}); });
    });
    st16_sc1<0>(l16, ob, v4u{pk2(ot[0][0][0], ot[0][0][1]), pk2(ot[0][0][2], ot[0][0][3]), pk2(ot[1][0][0], ot[1][0][1]), pk2(ot[1][0][2], ot[1][0][3])});
    const v4u d4 = v4u{pk2(ot[0][1][0], ot[0][1][1]), pk2(ot[0][1][2], ot[0][1][3]), pk2(ot[1][1][0], ot[1][1][1]), pk2(ot[1][1][2], ot[1][1][3])};
    if (lane < 16) st16_sc1<1024>(l16, ob, d4);
    if (lane == 0) __hip_atomic_fetch_add((LAS unsigned*)(lds + L_CTL), 1u, __ATOMIC_RELAXED, __HIP_MEMORY_SCOPE_WORKGROUP);
    issueA2(IC<2>{});
  }
  f32x4 acc2[4][5];
  sfor<0, 4>([&](auto M) { sfor<0, 5>([&](auto N) { acc2[M][N] = f32x4{0, 0, 0, 0}; }); });
  const unsigned probe = ((lane >> 2) & 3) * 69632 + 49152 + (4 * s + (lane & 3)) * 1280 + 1024 + 240;
  auto spin_ge = [&](int off, unsigned want) {
    for (unsigned spin = 0; spin < SPIN_CAP * 16; ++spin) {
      const unsigned f = *(volatile LAS unsigned*)(lds + off);
      if (__builtin_amdgcn_readfirstlane(f) >= want) break;
      __builtin_amdgcn_s_sleep(1);
    }
    asm volatile("" ::: "memory");
  };
  auto scale = [&]() {
    sfor<0, 4>([&](auto M) {
      const float og = *(const LAS float*)(lds + L_G + (64 * hf2 + 16 * M + r16) * 8 + 4);
      sfor<0, 5>([&](auto N) { acc2[M][N] *= og; });
    });
  };
  auto xaddr = [&](bool so, int sv, int dm, int n) {
    const int t = so ? dm * 5 + n - 3 : dm * 5 + n - (dm > 0 ? 2 : 0);
    return so ? (t < 15 ? sv * L_RO + t * 1024 : L_XO + sv * 2048 + (t - 15) * 1024) : L_RH + sv * 18432 + t * 1024;
  };
  auto dump = [&](auto M) {
    const int dm = (M - s) & 3;
    sfor<0, 5>([&](auto N) {
      if (dm != 0 || (OW ? N >= 3 : N < 3)) *(LAS f32x4*)(lds + xaddr(OW, s, dm, N) + l16) = acc2[M][N];
    });
  };
  sfor<0, NK>([&](auto I) {
    constexpr int i = I;
    if constexpr (i == NH) {
      wait_vm_pin<SC.wpo>(pollv);
      for (unsigned spin = 0; spin < SPIN_CAP && __any(pollv == SENT); ++spin) {
        __builtin_amdgcn_s_sleep(4);
        pollv = ld4_sc1_wait(probe, ab);
      }
      sfor<NH, NK>(issueA2);
      scale();
    }
    if constexpr (i == NK - 1) spin_ge(L_CTL, 8u);
    wait_vm<SC.w2[i]>();
    pin4(af[i][0], af[i][1], af[i][2], af[i][3]);
    if constexpr (i == NH - 1) ld4_sc1(pollv, probe, ab);
    pin5(bv[i][0], bv[i][1], bv[i][2], bv[i][3], bv[i][4]);
    if constexpr (i >= NH) {
      for (unsigned spin = 0; spin < SPIN_CAP; ++spin) {
        unsigned m = 0;
        sfor<0, 4>([&](auto M) { m = max(max(m, af[i][M][0]), max(max(af[i][M][1], af[i][M][2]), af[i][M][3])); });
        if (__builtin_expect(!__any(m == SENT), 1)) break;
        __builtin_amdgcn_s_sleep(2);
        issueA2(I);
        wait_vm<0>();
        pin4(af[i][0], af[i][1], af[i][2], af[i][3]);
      }
    }
    SB();
    sfor<0, 4>([&](auto M) {
      sfor<0, 5>([&](auto N) { acc2[M][N] = mfma(bv[i][N], af[i][M], acc2[M][N]); });
      SB();
      sfor<0, 3>([&](auto Kq) {
        constexpr Op o = SC.c[i][M][Kq];
        if constexpr (o.t == BV) issueBv(IC<o.a>{});
        if constexpr (o.t == A2) issueA2m(IC<o.a>{}, IC<o.b>{});
      });
      if constexpr (i == NK - 1 && M >= 1) dump(IC<M - 1>{});
      SB();
    });
  });
  dump(IC<3>{});
  {
    constexpr int N0 = OW ? 0 : 3, NN = OW ? 3 : 2;
    f32x4 so[NN];
    const int row = 64 * hf2 + 16 * s + r16;
    const float og = *(const LAS float*)(lds + L_G + row * 8 + 4);
    sfor<0, NN>([&](auto N) {
      const f32x4 own = s == 0 ? acc2[0][N0 + N] : s == 1 ? acc2[1][N0 + N] : s == 2 ? acc2[2][N0 + N] : acc2[3][N0 + N];
      const v2u b = lds8(lds, L_B2 + (N0 + N) * 32 + g * 8);
      so[N] = own + f32x4{bflo(b[0]), bfhi(b[0]), bflo(b[1]), bfhi(b[1])} * og;
    });
    u16* op = p.out + (size_t)(rb * 128 + row) * 640 + ct * 80 + N0 * 16 + g * 4;
    lds_bar();
    sfor<0, 4>([&](auto Dm) {
      const int sv = (s - Dm) & 3;
      sfor<0, NN>([&](auto N) {
        if constexpr (Dm != 0 || !OW) so[N] += __builtin_bit_cast(f32x4, lds16(lds, xaddr(true, sv, Dm, N0 + N) + l16));
        if constexpr (Dm != 0 || OW) so[N] += __builtin_bit_cast(f32x4, lds16(lds, xaddr(false, sv, Dm, N0 + N) + l16));
      });
    });
    sfor<0, NN>([&](auto N) { *(v2u*)(op + N * 16) = v2u{pk2(so[N][0], so[N][1]), pk2(so[N][2], so[N][3])}; });
  }
}

__global__ __launch_bounds__(512, 2) void fused_kernel(Params p) {
  extern __shared__ __attribute__((aligned(16))) char smem[];
  ldsp lds = (ldsp)smem;
  const int tid = threadIdx.x, lane = tid & 63;
  const int w = __builtin_amdgcn_readfirstlane(tid >> 6);
  const int b = blockIdx.x, xcd = b & 7, idx = b >> 3;
  const int rb = (xcd >> 2) * 8 + (idx & 7), cg = (xcd & 3) * 4 + (idx >> 3);
  if (w < 4) {
    body<0>(p, lds, w, lane, rb, cg);
  } else {
    body<1>(p, lds, w, lane, rb, cg);
  }
}

}

extern "C" void kernel_launch(void* const* d_in, const int* in_sizes, int n_in, void* d_out, int out_size, void* d_ws, size_t ws_size,
                              hipStream_t stream) {
  Params p;
  p.x = (const u16*)d_in[0];
  p.wqkv = (const u16*)d_in[1];
  p.wo = (const u16*)d_in[2];
  p.w1 = (const u16*)d_in[4];
  p.b1 = (const u16*)d_in[5];
  p.w2 = (const u16*)d_in[6];
  p.b2 = (const u16*)d_in[7];
  p.gw = (const u16*)d_in[8];
  p.out = (u16*)d_out;
  p.ws = (char*)d_ws;
  (void)hipFuncSetAttribute((const void*)fused_kernel, hipFuncAttributeMaxDynamicSharedMemorySize, LDS_BYTES);
  hipLaunchKernelGGL(fused_kernel, dim3(256), dim3(512), LDS_BYTES, stream, p);
}
```
